```python
import torch

def _softmax(x, axis=-1):
    return torch.softmax(x, dim=axis)
def _gelu(x):
    return torch.nn.functional.gelu(x, approximate="tanh")
def _silu(x):
    return torch.nn.functional.silu(x)
def _sigmoid(x):
    return torch.sigmoid(x)
def _layernorm(x, axis=-1, eps=1e-5):
    m = x.mean(dim=axis, keepdim=True)
    v = x.var(dim=axis, keepdim=True, unbiased=False)
    return (x - m) / torch.sqrt(v + eps)
def _scatter_add(tgt, idx, src, axis=0):
    return tgt.clone().index_add_(axis, idx, src)
def _segment_sum(src, seg, n):
    out = torch.zeros((n,) + src.shape[1:], dtype=src.dtype)
    return out.index_add_(0, seg, src)
def _pad(x, pw):
    flat = tuple(v for pair in reversed(pw) for v in pair)
    return torch.nn.functional.pad(x, flat)

kernel_name = 'concat_heads__ssm_conv_s4d__ssm_conv_s4__prompt'

def setup_inputs():

    _w = (lambda a: (lambda t: t.to(torch.bfloat16) if t.is_floating_point() else t)(torch.as_tensor(a)))
    def _A_setup():
        Bsz, L, D, N = 1, 2048, 640, 192
        return {
            "u": _w((lambda *s: torch.randn(*s, dtype=torch.bfloat16) * 0.05)(Bsz, L, D)),
            "log_A_real": _w((lambda *s: torch.randn(*s, dtype=torch.bfloat16) * 6.4)(D, N) * 0.5),
            "A_imag": _w((lambda *s: torch.randn(*s, dtype=torch.bfloat16) * 0.05)(D, N) * 3.14),
            "C_real": _w((lambda *s: torch.randn(*s, dtype=torch.bfloat16) * 0.05)(D, N) / N ** 0.5),
            "C_imag": _w((lambda *s: torch.randn(*s, dtype=torch.bfloat16) * 0.05)(D, N) / N ** 0.5),
            "log_dt": _w((lambda *s: torch.randn(*s, dtype=torch.bfloat16) * 0.05)(D) * 0.5 - 4.0),
            "D_skip": _w((lambda *s: torch.randn(*s, dtype=torch.bfloat16) * 0.05)(D)),
        }
    _A_in = _A_setup()

    def _B_setup():
        B, L, D, N = 1, 2048, 640, 192
        A_re = -(lambda _x: torch.exp(torch.as_tensor(_x)))((lambda *s: torch.randn(*s, dtype=torch.bfloat16) * 6.4)(D, N))
        A_im = torch.pi * (lambda *s: torch.randn(*s, dtype=torch.bfloat16) * 0.05)(D, N)
        B_re = (lambda *s: torch.randn(*s, dtype=torch.bfloat16) * 0.05)(D, N)
        B_im = (lambda *s: torch.randn(*s, dtype=torch.bfloat16) * 0.05)(D, N)
        C_re = (lambda *s: torch.randn(*s, dtype=torch.bfloat16) * 0.05)(D, N)
        C_im = (lambda *s: torch.randn(*s, dtype=torch.bfloat16) * 0.05)(D, N)
        log_dt = (lambda *s: torch.randn(*s, dtype=torch.bfloat16) * 0.05)(D) * 0.5 - 4.0
        Dskip = (lambda *s: torch.randn(*s, dtype=torch.bfloat16) * 0.05)(D)
        return {
            "u": _w((lambda *s: torch.randn(*s, dtype=torch.bfloat16) * 0.05)(B, L, D)),
            "A_re": _w(A_re),
            "A_im": _w(A_im),
            "B_re": _w(B_re),
            "B_im": _w(B_im),
            "C_re": _w(C_re),
            "C_im": _w(C_im),
            "log_dt": _w(log_dt),
            "Dskip": _w(Dskip),
        }
    _B_in = _B_setup()

    return {
        "A_u": _A_in["u"],
        "A_log_A_real": _A_in["log_A_real"],
        "A_A_imag": _A_in["A_imag"],
        "A_C_real": _A_in["C_real"],
        "A_C_imag": _A_in["C_imag"],
        "A_log_dt": _A_in["log_dt"],
        "A_D_skip": _A_in["D_skip"],
        "B_u": _B_in["u"],
        "B_A_re": _B_in["A_re"],
        "B_A_im": _B_in["A_im"],
        "B_B_re": _B_in["B_re"],
        "B_B_im": _B_in["B_im"],
        "B_C_re": _B_in["C_re"],
        "B_C_im": _B_in["C_im"],
        "B_log_dt": _B_in["log_dt"],
        "B_Dskip": _B_in["Dskip"],
    }

def reference(A_u, A_log_A_real, A_A_imag, A_C_real, A_C_imag, A_log_dt, A_D_skip, B_u, B_A_re, B_A_im, B_B_re, B_B_im, B_C_re, B_C_im, B_log_dt, B_Dskip):
    def _A_ref(u, log_A_real, A_imag, C_real, C_imag, log_dt, D_skip):
        L = u.shape[1]
        dt = torch.exp(log_dt)
        A = -torch.exp(log_A_real) + 1j * A_imag
        C = (C_real + 1j * C_imag) * dt[:, None]
        pos = (lambda x, dt: x.to(dt))(torch.arange(L), log_dt.dtype)
        P = torch.exp((A * dt[:, None])[:, :, None] * pos[None, None, :])
        K = 2.0 * torch.einsum("dn,dnl->dl", C, P).real
        L2 = 2 * L
        u_t = (lambda x, *d: x.permute(*d))(u, 0, 2, 1)
        Kf = (lambda x, *a, **k: torch.fft.rfft(x.float(), *a, **k))(K, L2)
        Uf = (lambda x, *a, **k: torch.fft.rfft(x.float(), *a, **k))(u_t, L2)
        y = (lambda x, *a, **k: torch.fft.irfft(x, *a, **k).to(torch.bfloat16))(Uf * Kf[None, :, :], L2)[:, :, :L]
        y = (lambda x, *d: x.permute(*d))(y, 0, 2, 1)
        return y + u * D_skip[None, None, :]

    def _B_ref(u, A_re, A_im, B_re, B_im, C_re, C_im, log_dt, Dskip):
        Bsz, L, D = u.shape
        dt = torch.exp(log_dt)[:, None]
        A = A_re + 1j * A_im
        Bc = B_re + 1j * B_im
        Cc = C_re + 1j * C_im
        logdA = dt * A
        dB = dt * Bc
        l = (lambda x, dt: x.to(dt))(torch.arange(L), A_re.dtype)
        P = torch.exp(logdA[:, :, None] * l[None, None, :])
        K = torch.einsum("dn,dnl,dn->dl", Cc, P, dB).real
        L2 = 2 * L
        Kp = _pad(K, ((0, 0), (0, L)))
        up = (lambda x, *d: x.permute(*d))(u, 0, 2, 1)
        up = _pad(up, ((0, 0), (0, 0), (0, L)))
        Kf = (lambda x, axes=(-2,-1): torch.fft.rfft2(x.float(), dim=axes))(Kp[:, None, :], axes=(-2, -1))
        Uf = (lambda x, axes=(-2,-1): torch.fft.rfft2(x.float(), dim=axes))(up[:, :, None, :], axes=(-2, -1))
        Yf = Uf * Kf[None, :, :, :]
        y = (lambda x, s, axes=(-2,-1): torch.fft.irfft2(x, s=s, dim=axes).to(torch.bfloat16))(Yf, (1, L2), axes=(-2, -1))[:, :, 0, :L]
        y = (lambda x, *d: x.permute(*d))(y, 0, 2, 1) + u * Dskip[None, None, :]
        return y

    a = _A_ref(A_u, A_log_A_real, A_A_imag, A_C_real, A_C_imag, A_log_dt, A_D_skip)
    b = _B_ref(B_u, B_A_re, B_A_im, B_B_re, B_B_im, B_C_re, B_C_im, B_log_dt, B_Dskip)
    return torch.concatenate([a, b], axis=-1)
```

```cpp
#include <hip/hip_runtime.h>
#include <hip/hip_bf16.h>
#include <stdint.h>

#define L_ 2048
#define D_ 640
#define N_ 192

__device__ __forceinline__ float bf2f(unsigned short v) { return __uint_as_float(((unsigned)v) << 16); }
__device__ __forceinline__ unsigned short f2bf(float f) {
    unsigned u = __float_as_uint(f);
    u += 0x7FFF + ((u >> 16) & 1);
    return (unsigned short)(u >> 16);
}
__device__ __forceinline__ float rbf(float f) { return bf2f(f2bf(f)); }

__global__ void kgen(const unsigned short* logAre, const unsigned short* Aim, const unsigned short* Cre,
                     const unsigned short* Cim, const unsigned short* logdtA,
                     const unsigned short* BAre, const unsigned short* BAim, const unsigned short* BBre,
                     const unsigned short* BBim, const unsigned short* BCre, const unsigned short* BCim,
                     const unsigned short* logdtB, float* K) {
    int ch = blockIdx.x;
    __shared__ float zr[N_], zi[N_], cr[N_], ci[N_];
    int n = threadIdx.x;
    if (n < N_) {
        if (ch < D_) {
            int d = ch;
            float dt = rbf(expf(bf2f(logdtA[d])));
            float ar = -rbf(expf(bf2f(logAre[d * N_ + n])));
            float ai = bf2f(Aim[d * N_ + n]);
            zr[n] = ar * dt; zi[n] = ai * dt;
            cr[n] = 2.f * bf2f(Cre[d * N_ + n]) * dt; ci[n] = 2.f * bf2f(Cim[d * N_ + n]) * dt;
        } else {
            int d = ch - D_;
            float dt = rbf(expf(bf2f(logdtB[d])));
            float ar = bf2f(BAre[d * N_ + n]);
            float ai = bf2f(BAim[d * N_ + n]);
            zr[n] = ar * dt; zi[n] = ai * dt;
            float br = bf2f(BBre[d * N_ + n]) * dt, bi = bf2f(BBim[d * N_ + n]) * dt;
            float c_r = bf2f(BCre[d * N_ + n]), c_i = bf2f(BCim[d * N_ + n]);
            cr[n] = c_r * br - c_i * bi; ci[n] = c_r * bi + c_i * br;
        }
    }
    __syncthreads();
    for (int l = threadIdx.x; l < L_; l += blockDim.x) {
        float p = rbf((float)l);
        float acc = 0.f;
        for (int k = 0; k < N_; ++k) {
            float e = expf(zr[k] * p);
            float s, c;
            sincosf(zi[k] * p, &s, &c);
            acc += e * (cr[k] * c - ci[k] * s);
        }
        K[ch * L_ + l] = acc;
    }
}

__global__ void conv(const unsigned short* uA, const unsigned short* uB, const unsigned short* DA,
                     const unsigned short* DB, const float* K, unsigned short* out) {
    int ch = blockIdx.x;
    __shared__ float ks[L_], us[L_];
    const unsigned short* u = ch < D_ ? uA : uB;
    int d = ch < D_ ? ch : ch - D_;
    float dsk = bf2f(ch < D_ ? DA[d] : DB[d]);
    for (int l = threadIdx.x; l < L_; l += blockDim.x) { ks[l] = K[ch * L_ + l]; us[l] = bf2f(u[l * D_ + d]); }
    __syncthreads();
    for (int t = threadIdx.x; t < L_; t += blockDim.x) {
        float acc = 0.f;
        for (int j = 0; j <= t; ++j) acc += ks[t - j] * us[j];
        float y = rbf(acc);
        float sk = rbf(us[t] * dsk);
        out[t * (2 * D_) + ch] = f2bf(y + sk);
    }
}

extern "C" void kernel_launch(void* const* d_in, const int* in_sizes, int n_in,
                              void* d_out, int out_size, void* d_ws, size_t ws_size,
                              hipStream_t stream) {
    typedef const unsigned short* P;
    float* K = (float*)d_ws;
    kgen<<<2 * D_, 256, 0, stream>>>((P)d_in[1], (P)d_in[2], (P)d_in[3], (P)d_in[4], (P)d_in[5],
                                     (P)d_in[8], (P)d_in[9], (P)d_in[10], (P)d_in[11], (P)d_in[12], (P)d_in[13],
                                     (P)d_in[14], K);
    conv<<<2 * D_, 256, 0, stream>>>((P)d_in[0], (P)d_in[7], (P)d_in[6], (P)d_in[15], K, (unsigned short*)d_out);
}
```

```cpp
#include <hip/hip_runtime.h>
#include <stdint.h>

typedef float f2 __attribute__((ext_vector_type(2)));
typedef float f4 __attribute__((ext_vector_type(4)));
typedef float f16v __attribute__((ext_vector_type(16)));
typedef _Float16 h2 __attribute__((ext_vector_type(2)));
typedef _Float16 h8 __attribute__((ext_vector_type(8)));
typedef __bf16 b2 __attribute__((ext_vector_type(2)));
typedef __bf16 b8 __attribute__((ext_vector_type(8)));
typedef unsigned u2 __attribute__((ext_vector_type(2)));
typedef unsigned u4 __attribute__((ext_vector_type(4)));
typedef unsigned short us2 __attribute__((ext_vector_type(2)));
typedef const unsigned short* cusp;

#define LDS __attribute__((address_space(3)))
#define DI __device__ __forceinline__

constexpr int LL = 2048, DD = 640, NN = 192;
constexpr int PAD = 272;
constexpr int USTR = (PAD + LL) * 2;
constexpr int U_OFF = 0;
constexpr int SROW = 272;
constexpr int SSTR = 8720;
constexpr int S_OFF = U_OFF + 8 * USTR;
constexpr int KODD = 304;
constexpr int KFAR = 592;
constexpr int KSTR = (KFAR + 6 * 96) * 4;
constexpr int K_OFF = S_OFF + 8 * SSTR;
constexpr int LDS_BYTES = K_OFF + 8 * KSTR;

#define STAMP(i)

DI float bfl(unsigned v) { return __uint_as_float(v << 16); }
DI float bfh(unsigned v) { return __uint_as_float(v & 0xffff0000u); }
DI unsigned pkbf(float lo, float hi) { f2 v = {lo, hi}; return __builtin_bit_cast(unsigned, __builtin_convertvector(v, b2)); }
DI float rbf(float x) { return __uint_as_float(pkbf(0.f, x)); }

DI f2 cmul(f2 x, f2 y) {
    f2 t, r;
    asm("v_pk_mul_f32 %0, %1, %2 op_sel_hi:[0,1]" : "=v"(t) : "v"(x), "v"(y));
    asm("v_pk_fma_f32 %0, %1, %2, %3 op_sel:[1,1,0] op_sel_hi:[1,0,1] neg_lo:[0,1,0]" : "=v"(r) : "v"(x), "v"(y), "v"(t));
    return r;
}
DI h2 cmulh(h2 x, h2 y) {
    h2 t = x.xx * y;
    h2 ys = {-y.y, y.x};
    return __builtin_elementwise_fma(x.yy, ys, t);
}
DI h2 cvth(f2 x) { return __builtin_convertvector(x, h2); }
DI h2 cvthc(f2 x) {
    h2 r;
    asm("v_cvt_pk_f16_f32 %0, %1, -%2" : "=v"(r) : "v"(x.x), "v"(x.y));
    return r;
}
DI f2 cexp2r(float a2, float b2r) {
    float m = __builtin_amdgcn_exp2f(a2);
    float fr = __builtin_amdgcn_fractf(b2r);
    float s = __builtin_amdgcn_sinf(fr), c = __builtin_amdgcn_cosf(fr);
    return f2{m * c, m * s};
}
DI void lds_barrier() { asm volatile("s_waitcnt lgkmcnt(0)\n\ts_barrier" ::: "memory"); }

DI unsigned band_dw(int v0, int wd) {
    unsigned lo = ((unsigned)(v0) < (unsigned)wd) ? 0x3F80u : 0u;
    unsigned hi = ((unsigned)(v0 - 1) < (unsigned)wd) ? 0x3F800000u : 0u;
    return lo | hi;
}
DI u4 band_frag(int v, int wd) {
    return u4{band_dw(v, wd), band_dw(v - 2, wd), band_dw(v - 4, wd), band_dw(v - 6, wd)};
}
DI f4 mfma16(u4 a, u4 b, f4 c) {
    return __builtin_amdgcn_mfma_f32_16x16x32_bf16(__builtin_bit_cast(b8, a), __builtin_bit_cast(b8, b), c, 0, 0, 0);
}
DI u2 pk4(f4 d) { return u2{pkbf(d[0], d[1]), pkbf(d[2], d[3])}; }
DI u4 cat(u2 a, u2 b) { return u4{a.x, a.y, b.x, b.y}; }

struct Args {
    cusp u[2];
    cusp p[2][6];
    cusp logdt[2];
    cusp dskip[2];
    unsigned short* out;
    unsigned long long* stamps;
};

__global__ __launch_bounds__(512, 2) void ssm_kernel(Args A) {
    __shared__ __attribute__((aligned(16))) char smem[LDS_BYTES];
    LDS char* const lds = (LDS char*)smem;
    const int tid = threadIdx.x, lane = tid & 63;
    const int w = __builtin_amdgcn_readfirstlane(tid >> 6);
    const int grp = (blockIdx.x & 7) * 20 + (blockIdx.x >> 3);
    const int br = grp >= 80;
    const int d0 = (grp - br * 80) * 8;
    const int d = d0 + w;
    STAMP(0);

    unsigned pw[6]; unsigned short ps[6];
#pragma unroll
    for (int i = 0; i < 6; ++i) {
        if (i < 4 || br) {
            cusp p = A.p[br][i] + d * NN;
            pw[i] = *(const unsigned*)(p + 2 * lane);
            ps[i] = p[128 + lane];
        } else { pw[i] = 0; ps[i] = 0; }
    }
    const unsigned short ldt = A.logdt[br][d];
    const unsigned short dsk = A.dskip[br][d];
    u4 ur[4];
    {
        cusp ub = A.u[br] + (size_t)(256 * w + 4 * lane) * DD + d0;
#pragma unroll
        for (int r = 0; r < 4; ++r) ur[r] = *(const u4*)(ub + r * DD);
    }

    LDS char* const ubase = lds + U_OFF + w * USTR;
    LDS char* const sbase = lds + S_OFF + w * SSTR;
    LDS char* const kbase = lds + K_OFF + w * KSTR;
    {
        u4 z = {0, 0, 0, 0};
        if (lane < PAD * 2 / 16) *(LDS u4*)(ubase + lane * 16) = z;
#pragma unroll
        for (int i = 0; i < 5; ++i)
            if (i * 64 + lane < KSTR / 16) *(LDS u4*)(kbase + (i * 64 + lane) * 16) = z;
    }
    const int c16 = lane & 15, g4 = lane >> 4;
    const int vb = c16 - 8 * g4;
    const u4 bandA = band_frag(vb - 3 + 16, 3), bandB = band_frag(vb - 6 + 16, 5), bandC = band_frag(vb - 11 + 16, 3);
    const u4 bandD = band_frag(vb - 14 + 24, 5), bandE = band_frag(vb - 12 + 24, 9), bandF = band_frag(vb - 5 + 16, 7);
    const u4 bandI = band_frag(vb + 10, 7);
    const u4 bandS = band_frag(vb + 16, 1);

    const float dt = rbf(expf(bfl(ldt)));
    float a2[3], b2r[3]; f2 cc[3];
    unsigned mx = 0;
#pragma unroll
    for (int un = 0; un < 3; ++un) {
        float v[6];
#pragma unroll
        for (int i = 0; i < 6; ++i) v[i] = un == 0 ? bfl(pw[i]) : un == 1 ? bfh(pw[i]) : bfl(ps[i]);
        float are = br ? v[0] : -rbf(__builtin_amdgcn_exp2f(v[0] * 1.44269504f));
        a2[un] = (are * dt) * 1.44269504f;
        b2r[un] = (v[1] * dt) * 0.159154943f;
        if (br) {
            float xr = v[4] * dt, xi = v[5] * dt;
            cc[un] = f2{v[2] * xr - v[3] * xi, v[2] * xi + v[3] * xr};
        } else {
            float dt2 = dt + dt;
            cc[un] = f2{v[2] * dt2, v[3] * dt2};
        }
        unsigned m = __float_as_uint(fmaxf(fabsf(cc[un].x), fabsf(cc[un].y))) >> 23;
        float gr = fminf(fmaxf(a2[un] * 2048.f, 0.f), 128.f);
        m += (unsigned)ceilf(gr);
        mx = max(mx, m);
    }
    mx = max(mx, (unsigned)__builtin_amdgcn_update_dpp(0, (int)mx, 0x128, 0xf, 0xf, false));
    mx = max(mx, (unsigned)__builtin_amdgcn_update_dpp(0, (int)mx, 0x124, 0xf, 0xf, false));
    mx = max(mx, (unsigned)__builtin_amdgcn_update_dpp(0, (int)mx, 0x122, 0xf, 0xf, false));
    mx = max(mx, (unsigned)__builtin_amdgcn_update_dpp(0, (int)mx, 0x121, 0xf, 0xf, false));
    int em = max(max(__builtin_amdgcn_readlane((int)mx, 0), __builtin_amdgcn_readlane((int)mx, 16)),
                 max(__builtin_amdgcn_readlane((int)mx, 32), __builtin_amdgcn_readlane((int)mx, 48)));
    em = min(max(em, 14), 266);
    const float scl = __int_as_float((267 - em) << 23);
    const float iscl = __int_as_float((em - 13) << 23);
    STAMP(1);

    f16v kacc;
#pragma unroll
    for (int i = 0; i < 16; ++i) kacc[i] = 0.f;
    LDS unsigned* const srow = (LDS unsigned*)(sbase + lane * 4);
    const int r32 = lane & 31, hh = lane >> 5;
    LDS char* const fragT = sbase + r32 * SROW + hh * 16;
    LDS char* const fragR = sbase + (r32 < 24 ? r32 : r32 - 8) * SROW + hh * 16;
#define WROW(r, val) srow[(r) * (SROW / 4)] = __builtin_bit_cast(unsigned, val)
#pragma unroll 1
    for (int un = 0; un < 3; ++un) {
        const float a = un == 0 ? a2[0] : un == 1 ? a2[1] : a2[2];
        const float b = un == 0 ? b2r[0] : un == 1 ? b2r[1] : b2r[2];
        const f2 c = (un == 0 ? cc[0] : un == 1 ? cc[1] : cc[2]) * scl;
        const f2 w1 = cexp2r(a, b), w64 = cexp2r(a * 64.f, b * 64.f);
        const f2 w2 = cmul(w1, w1), w128 = cmul(w64, w64);
        const f2 w4 = cmul(w2, w2), w192 = cmul(w128, w64), w256 = cmul(w128, w128);
        const f2 w8 = cmul(w4, w4), w6 = cmul(w4, w2);
        const f2 w16 = cmul(w8, w8);
        const f2 w24 = cmul(w16, w8), w32 = cmul(w16, w16);
        const f2 w40 = cmul(w32, w8), w48 = cmul(w32, w16), w56 = cmul(w32, w24);
        h2 t[8], T8 = cvthc(w64), T16 = cvthc(w128), T24 = cvthc(w192);
        t[1] = cvthc(w8); t[2] = cvthc(w16); t[3] = cvthc(w24); t[4] = cvthc(w32);
        t[5] = cvthc(w40); t[6] = cvthc(w48); t[7] = cvthc(w56);
        WROW(0, 0x3C00u); WROW(8, T8); WROW(16, T16); WROW(24, T24);
#pragma unroll
        for (int j = 1; j < 8; ++j) {
            WROW(j, t[j]);
            WROW(8 + j, cmulh(T8, t[j])); WROW(16 + j, cmulh(T16, t[j])); WROW(24 + j, cmulh(T24, t[j]));
        }
        h8 tf[8];
#pragma unroll
        for (int s = 0; s < 8; ++s) tf[s] = *(LDS h8*)(fragT + 32 * s);
        const h2 h2w = cvth(w2), h4w = cvth(w4), h6w = cvth(w6);
        const f2 c1 = cmul(c, w1), c256 = cmul(c, w256);
        const f2 c512 = cmul(c256, w256), c768 = cmul(c512, w256), c1024 = cmul(c768, w256), c1280 = cmul(c1024, w256);
        const f2 c1536 = cmul(c1280, w256), c1792 = cmul(c1536, w256), c2048 = cmul(c1792, w256);
        const h2 r0 = cvth(c), r1 = cvth(c1), r256 = cvth(c256), r512 = cvth(c512), r768 = cvth(c768);
        WROW(0, r0); WROW(1, r1); WROW(2, cmulh(r0, h2w)); WROW(3, cmulh(r1, h2w));
        WROW(8, cmulh(r0, h4w)); WROW(9, cmulh(r1, h4w)); WROW(10, cmulh(r0, h6w)); WROW(11, cmulh(r1, h6w));
        WROW(4, r256); WROW(5, cmulh(r256, h2w)); WROW(6, cmulh(r256, h4w)); WROW(7, cmulh(r256, h6w));
        WROW(20, r512); WROW(21, cmulh(r512, h4w)); WROW(22, r768); WROW(23, cmulh(r768, h4w));
        WROW(16, cvth(c1024)); WROW(17, cvth(c1536)); WROW(18, cvth(c1280)); WROW(19, cvth(c1792));
        WROW(12, cvth(c2048));
#pragma unroll
        for (int s = 0; s < 8; ++s) {
            h8 rf = *(LDS h8*)(fragR + 32 * s);
            kacc = __builtin_amdgcn_mfma_f32_32x32x16_f16(rf, tf[s], kacc, 0, 0, 0);
        }
    }
    STAMP(2);

    {
        LDS char* ud = lds + U_OFF + (PAD + 256 * w + 4 * lane) * 2;
#pragma unroll
        for (int k = 0; k < 4; ++k) {
            u2 lo = {__builtin_amdgcn_perm(ur[1][k], ur[0][k], 0x05040100u), __builtin_amdgcn_perm(ur[3][k], ur[2][k], 0x05040100u)};
            u2 hi = {__builtin_amdgcn_perm(ur[1][k], ur[0][k], 0x07060302u), __builtin_amdgcn_perm(ur[3][k], ur[2][k], 0x07060302u)};
            *(LDS u2*)(ud + (2 * k) * USTR) = lo;
            *(LDS u2*)(ud + (2 * k + 1) * USTR) = hi;
        }
    }
    STAMP(3);

    float k1024;
    {
        float kv[12];
#pragma unroll
        for (int i = 0; i < 12; ++i) kv[i] = kacc[i] * iscl;
        const float k512 = __int_as_float(__builtin_amdgcn_readlane(__float_as_int(kv[8]), 32));
        k1024 = __int_as_float(__builtin_amdgcn_readlane(__float_as_int(kv[8]), 0));
        float nx = __int_as_float(__builtin_amdgcn_update_dpp(__float_as_int(k512), __float_as_int(kv[0]), 0x130, 0xf, 0xf, false));
        const bool up = lane >= 32;
        const float x0 = kv[0], x1 = up ? kv[0] : kv[1], x2 = up ? kv[1] : kv[2], x3 = up ? kv[2] : kv[3];
        const float x4 = up ? kv[2] : kv[4], x5 = up ? kv[2] : kv[5], x6 = up ? kv[3] : kv[6], x7 = up ? nx : kv[7];
        LDS unsigned* kd = (LDS unsigned*)kbase;
        *(LDS u4*)(kd + 268 - 4 * lane) = u4{pkbf(x7, x6), pkbf(x5, x4), pkbf(x3, x2), pkbf(x1, x0)};
        *(LDS u4*)(kd + KODD + 268 - 4 * lane) = u4{pkbf(nx, x7), pkbf(x6, x5), pkbf(x4, x3), pkbf(x2, x1)};
        if (lane == 63) {
            unsigned kk = pkbf(k512, k512);
            kd[14] = kk & 0xffff0000u; kd[15] = kk; kd[KODD + 15] = kk;
        }
        if (lane == 0) kd[KODD + 272] = pkbf(x0, 0.f);
        const int b = lane & 31, bo = b & 1, bh = b >> 1;
        int comp0, comp1, xa, xb;
        if (up) { comp0 = bo ? 1 : 3; comp1 = bo ? 2 : 0; xa = 47 - (bo ? bh : bh - 1); xb = 47 - bh; }
        else    { comp0 = comp1 = bo ? 5 : 4; xa = (bo ? 63 : 64) - bh; xb = xa - 32; }
        LDS unsigned short* fa = (LDS unsigned short*)(kd + KFAR + 96 * comp0) + xa;
        LDS unsigned short* fb = (LDS unsigned short*)(kd + KFAR + 96 * comp1) + xb;
        const unsigned p89 = pkbf(b == 0 ? 0.f : kv[8], kv[9]), pAB = pkbf(kv[10], kv[11]);
        fa[0] = (unsigned short)p89;         fa[97] = (unsigned short)p89;
        fb[0] = (unsigned short)(p89 >> 16); fb[97] = (unsigned short)(p89 >> 16);
        fa[-16] = (unsigned short)pAB;         fa[97 - 16] = (unsigned short)pAB;
        fb[-16] = (unsigned short)(pAB >> 16); fb[97 - 16] = (unsigned short)(pAB >> 16);
        if (lane == 32) {
            LDS unsigned short* fz = (LDS unsigned short*)(kd + KFAR + 96 * 4);
            unsigned short v = (unsigned short)pkbf(kv[4], 0.f);
            fz[0] = v; fz[97] = v;
        }
    }
    lds_barrier();
    STAMP(4);

    LDS char* const uf = ubase + (PAD - 16) * 2 + c16 * 32 + g4 * 16;
#define UFRAG(pos) (*(LDS u4*)(uf + ((pos) + 16) * 2))
    LDS unsigned* const kn = (LDS unsigned*)kbase + ((527 - c16) >> 1) + 4 * g4 + ((c16 & 1) ? 0 : KODD + 1);
    LDS unsigned* const kf = (LDS unsigned*)kbase + KFAR + ((79 - c16) >> 1) + 2 * g4 + ((c16 & 1) ? 0 : 49);
    u4 KN[17];
#pragma unroll
    for (int m = 0; m < 17; ++m) KN[m] = u4{kn[-16 * m], kn[-16 * m + 1], kn[-16 * m + 2], kn[-16 * m + 3]};

    u4 S2p[6], S2q[6], S3[4];
    const f4 z4 = {0.f, 0.f, 0.f, 0.f};
#pragma unroll
    for (int x = 0; x < 6; ++x) {
        u4 f16_ = UFRAG(256 * x - 16), f24 = UFRAG(256 * x - 24);
        S2p[x] = cat(pk4(mfma16(f16_, bandA, z4)), pk4(mfma16(f16_, bandB, z4)));
        S2q[x] = cat(pk4(mfma16(f16_, bandC, z4)), pk4(mfma16(f24, bandD, z4)));
        if (x < 4) S3[x] = cat(pk4(mfma16(f24, bandE, z4)), pk4(mfma16(f16_, bandF, z4)));
    }
    u4 K2p[3], K2q[3], K3[4];
#pragma unroll
    for (int i = 0; i < 3; ++i) {
        const int o = -(32 + 16 * i) / 2;
        K2p[i] = u4{kf[o], kf[o + 1], kf[96 + o], kf[96 + o + 1]};
        K2q[i] = u4{kf[192 + o], kf[192 + o + 1], kf[288 + o], kf[288 + o + 1]};
    }
#pragma unroll
    for (int i = 0; i < 4; ++i) {
        const int o = 24 - (64 + 16 * i) / 2;
        K3[i] = u4{kf[384 + o], kf[384 + o + 1], kf[480 + o], kf[480 + o + 1]};
    }
    const unsigned kk1024 = pkbf(k1024, k1024), dd = (unsigned)dsk * 0x10001u;
    u4 KI, KS;
#pragma unroll
    for (int i = 0; i < 4; ++i) {
        unsigned mI = (bandI[i] >> 7) * 0x204u | (bandI[i] >> 7) * 0x204u >> 7;
        unsigned mS = (bandS[i] >> 7) * 0x204u | (bandS[i] >> 7) * 0x204u >> 7;
        mI = ((bandI[i] & 0xffffu) ? 0xffffu : 0u) | ((bandI[i] >> 16) ? 0xffff0000u : 0u);
        mS = ((bandS[i] & 0xffffu) ? 0xffffu : 0u) | ((bandS[i] >> 16) ? 0xffff0000u : 0u);
        KI[i] = mI & kk1024; KS[i] = mS & dd;
    }
    STAMP(5);

    LDS char* const yw = sbase + c16 * 32 + ((g4 ^ (c16 >> 2)) & 3) * 8;
    const int tl = 32 * w + (lane >> 1);
    LDS char* const yr = lds + S_OFF + (lane & 1) * 4 * SSTR + (tl >> 4) * 32 + ((((tl >> 2) ^ (tl >> 6)) & 3) * 8) + (tl & 3) * 2;
    unsigned short* const og = A.out + (size_t)tl * (2 * DD) + br * DD + d0 + (lane & 1) * 4;
    f4 acc[10];
#pragma unroll
    for (int i = 0; i < 10; ++i) acc[i] = z4;
#pragma unroll
    for (int T = 0; T < 8; ++T) {
        f4 sk;
#pragma unroll
        for (int m = 7; m >= 0; --m) {
            const int x = 8 * T - m;
            u4 f = UFRAG(32 * x - 16);
            acc[T] = mfma16(KN[m], f, acc[T]);
            if (T + 1 < 8) acc[T + 1] = mfma16(KN[m + 8], f, acc[T + 1]);
            if (m == 0 && T + 2 < 8) acc[T + 2] = mfma16(KN[16], f, acc[T + 2]);
            if (m == 0) sk = mfma16(KS, f, z4);
        }
#pragma unroll
        for (int i = 0; i < 3; ++i) {
            const int x = T - 2 - i;
            if (x >= 0) { acc[T] = mfma16(S2p[x], K2p[i], acc[T]); acc[T] = mfma16(S2q[x], K2q[i], acc[T]); }
        }
#pragma unroll
        for (int i = 0; i < 4; ++i) {
            const int x = T - 4 - i;
            if (x >= 0) acc[T] = mfma16(S3[x], K3[i], acc[T]);
        }
        if (T >= 3) acc[T] = mfma16(KI, UFRAG(256 * T - 1032), acc[T]);
        float r[4];
#pragma unroll
        for (int i = 0; i < 4; ++i) r[i] = rbf(acc[T][i]) + rbf(sk[i]);
        *(LDS u2*)(yw + T * 512) = u2{pkbf(r[0], r[1]), pkbf(r[2], r[3])};
        lds_barrier();
        us2 o0, o1;
        o0.x = *(LDS unsigned short*)(yr + T * 512);
        o0.y = *(LDS unsigned short*)(yr + T * 512 + SSTR);
        o1.x = *(LDS unsigned short*)(yr + T * 512 + 2 * SSTR);
        o1.y = *(LDS unsigned short*)(yr + T * 512 + 3 * SSTR);
        *(u2*)(og + (size_t)T * 256 * (2 * DD)) = u2{__builtin_bit_cast(unsigned, o0), __builtin_bit_cast(unsigned, o1)};
        STAMP(6 + T);
    }
}

extern "C" void kernel_launch(void* const* d_in, const int* in_sizes, int n_in,
                              void* d_out, int out_size, void* d_ws, size_t ws_size,
                              hipStream_t stream) {
    Args A;
    A.u[0] = (cusp)d_in[0]; A.u[1] = (cusp)d_in[7];
    A.p[0][0] = (cusp)d_in[1]; A.p[0][1] = (cusp)d_in[2]; A.p[0][2] = (cusp)d_in[3]; A.p[0][3] = (cusp)d_in[4];
    A.p[0][4] = (cusp)d_in[3]; A.p[0][5] = (cusp)d_in[4];
    A.p[1][0] = (cusp)d_in[8]; A.p[1][1] = (cusp)d_in[9]; A.p[1][2] = (cusp)d_in[12]; A.p[1][3] = (cusp)d_in[13];
    A.p[1][4] = (cusp)d_in[10]; A.p[1][5] = (cusp)d_in[11];
    A.logdt[0] = (cusp)d_in[5]; A.logdt[1] = (cusp)d_in[14];
    A.dskip[0] = (cusp)d_in[6]; A.dskip[1] = (cusp)d_in[15];
    A.out = (unsigned short*)d_out;
    A.stamps = (unsigned long long*)d_ws;
    ssm_kernel<<<160, 512, 0, stream>>>(A);
}
```

```cpp
#include <hip/hip_runtime.h>
#include <stdint.h>

struct Args {
    const void* p[2][8];
    const void* skip[2];
    void* out;
};

__global__ __launch_bounds__(512, 2) void ssm_kernel(Args A) {
    __shared__ __attribute__((aligned(16))) char smem[161696];
    unsigned tid = threadIdx.x;
    asm volatile(
    "s_and_b32 s8, %2, 7\n"
    "s_cmp_ge_u32 s8, 4\n"
    "s_cselect_b32 s15, 64, 0\n"
    "s_cselect_b32 s9, 1, 0\n"
    "s_load_dwordx16 s[16:31], %1, s15\n"
    "s_getpc_b64 s[54:55]\n"
    "v_lshlrev_b32 v1, 7, v0\n"
    "v_min_u32 v1, 23028, v1\n"
    "global_load_dword v1, v1, s[54:55]\n"
    "s_lshr_b32 s13, %2, 3\n"
    "s_and_b32 s14, s8, 3\n"
    "s_mul_i32 s14, s14, 20\n"
    "s_add_u32 s14, s14, s13\n"
    "s_cmp_ge_u32 s13, 20\n"
    "s_cbranch_scc1 .Lhelp%=\n"
    "s_mul_i32 s10, s14, 8\n"
    "s_min_u32 s10, s10, 632\n"
    "v_lshrrev_b32 v2, 6, v0\n"
    "v_and_b32 v3, 63, v0\n"
    "v_readfirstlane_b32 s11, v2\n"
    "s_mov_b32 s56, s11\n"
    "s_mov_b32 s57, s11\n"
    "s_add_u32 s12, s10, s56\n"
    "s_mul_i32 s13, s12, 384\n"
    "v_lshl_add_u32 v4, v3, 2, s13\n"
    "v_lshl_add_u32 v5, v3, 1, s13\n"
    "v_mov_b32 v6, 0\n"
    "v_mov_b32 v7, 0\n"
    "v_mov_b32 v8, 0\n"
    "v_mov_b32 v9, 0\n"
    "v_mov_b32 v10, 0\n"
    "v_mov_b32 v11, 0\n"
    "s_mul_i32 s38, s11, 8720\n"
    "s_mul_i32 s39, s56, 4640\n"
    "s_add_u32 s39, s39, 69760\n"
    "s_mul_i32 s40, s57, 6848\n"
    "s_add_u32 s40, s40, 106880\n"
    "s_add_u32 s38, s38, %3\n"
    "s_add_u32 s39, s39, %3\n"
    "s_add_u32 s40, s40, %3\n"
    "s_mov_b32 m0, s38\n"
    "s_add_u32 s59, %3, 161664\n"
    "s_lshl_b32 s13, s11, 2\n"
    "s_add_u32 s13, s13, s59\n"
    "v_mov_b32 v0, s13\n"
    "ds_write_b32 v0, v6\n"
    "v_and_b32 v2, 3, v3\n"
    "v_mov_b32 v12, v2\n"
    "v_lshl_add_u32 v2, v2, 2, s59\n"
    "s_mov_b32 s41, 0x05040100\n"
    "s_mov_b32 s42, 0x07060302\n"
    "s_mov_b64 s[44:45], 0\n"
    "s_mov_b32 s45, -1\n"
    "s_mov_b32 s46, 1\n"
    "s_mov_b32 s47, 1\n"
    "s_mov_b32 s48, 0x43000000\n"
    "s_mul_i32 s13, s11, 327680\n"
    "s_lshl_b32 s14, s10, 1\n"
    "s_add_u32 s13, s13, s14\n"
    "v_mul_u32_u24 v13, 5120, v3\n"
    "v_add_u32 v13, s13, v13\n"
    "v_and_b32 v14, 15, v3\n"
    "v_lshrrev_b32 v15, 4, v3\n"
    "v_lshlrev_b32 v16, 3, v15\n"
    "v_sub_u32 v17, v14, v16\n"
    "s_lshl_b32 s13, s11, 3\n"
    "s_mov_b32 s50, 0x1413161b\n"
    "s_mov_b32 s51, 0x20201415\n"
    "s_lshr_b64 s[50:51], s[50:51], s13\n"
    "s_and_b32 s50, s50, 0xff\n"
    "s_mov_b32 s52, 0x9030503\n"
    "s_mov_b32 s53, 0x1010707\n"
    "s_lshr_b64 s[52:53], s[52:53], s13\n"
    "s_bfm_b32 s52, s52, 0\n"
    "s_cmp_lt_u32 s11, 5\n"
    "s_cselect_b32 s51, 0x3f80, 1\n"
    "v_add_u32 v16, s50, v17\n"
    "v_med3_i32 v16, v16, 0, 31\n"
    "v_lshlrev_b32_e64 v16, v16, s52\n"
    "v_bfe_u32 v20, v16, 16, 2\n"
    "v_bfe_u32 v21, v16, 18, 2\n"
    "v_bfe_u32 v22, v16, 20, 2\n"
    "v_bfe_u32 v23, v16, 22, 2\n"
    "v_mul_u32_u24 v20, 0x8001, v20\n"
    "v_mul_u32_u24 v21, 0x8001, v21\n"
    "v_mul_u32_u24 v22, 0x8001, v22\n"
    "v_mul_u32_u24 v23, 0x8001, v23\n"
    "v_and_b32 v20, 0x10001, v20\n"
    "v_and_b32 v21, 0x10001, v21\n"
    "v_and_b32 v22, 0x10001, v22\n"
    "v_and_b32 v23, 0x10001, v23\n"
    "v_mul_u32_u24 v20, s51, v20\n"
    "v_mul_u32_u24 v21, s51, v21\n"
    "v_mul_u32_u24 v22, s51, v22\n"
    "v_mul_u32_u24 v23, s51, v23\n"
    "s_add_u32 s13, %3, 72352\n"
    "v_lshl_add_u32 v16, v3, 4, s13\n"
    "s_lshr_b32 s14, s11, 1\n"
    "s_mul_i32 s14, s14, 2592\n"
    "s_lshl_b32 s13, s11, 10\n"
    "s_add_u32 s14, s14, s13\n"
    "v_add_u32 v18, s14, v16\n"
    "ds_write_b128 v18, v[20:23]\n"
    "s_waitcnt lgkmcnt(0)\n"
    "global_load_dword v19, v4, s[16:17]\n"
    "global_load_dword v18, v4, s[18:19]\n"
    "s_lshl_b32 s58, s12, 1\n"
    "s_and_b32 s58, s58, -4\n"
    "s_load_dword s36, s[20:21], s58\n"
    "v_mov_b32 v24, 0\n"
    "v_mov_b32 v25, 0\n"
    "v_mov_b32 v26, 0\n"
    "v_mov_b32 v27, 0\n"
    "s_add_u32 s14, s40, 4864\n"
    "v_lshl_add_u32 v20, v3, 4, s14\n"
    "ds_write_b128 v20, v[24:27]\n"
    "ds_write_b128 v20, v[24:27] offset:960\n"
    "v_and_b32 v21, 7, v3\n"
    "v_bfe_u32 v22, v3, 3, 2\n"
    "v_cmp_lt_u32 vcc, 4, v21\n"
    "v_mul_u32_u24 v22, 1216, v22\n"
    "v_lshl_add_u32 v21, v21, 4, s40\n"
    "v_cndmask_b32_e64 v20, 0, 64, vcc\n"
    "v_add_u32 v21, v21, v22\n"
    "v_lshl_add_u32 v21, v20, 4, v21\n"
    "ds_write_b128 v21, v[24:27]\n"
    "v_min_u32 v23, 33, v3\n"
    "v_lshl_add_u32 v23, v23, 4, s39\n"
    "ds_write_b128 v23, v[24:27]\n"
    "v_and_b32 v28, 31, v3\n"
    "v_lshrrev_b32 v29, 5, v3\n"
    "v_lshl_add_u32 v30, v29, 4, s38\n"
    "s_movk_i32 s13, 272\n"
    "v_mad_u32_u24 v31, v28, s13, v30\n"
    "v_mov_b32 v30, 0x3c00\n"
    "s_lshl_b32 s13, s11, 9\n"
    "s_add_u32 s13, s13, 70304\n"
    "s_add_u32 s13, s13, %3\n"
    "v_lshl_add_u32 v20, v3, 3, s13\n"
    "v_lshlrev_b32 v21, 5, v14\n"
    "v_lshl_add_u32 v22, v15, 4, s39\n"
    "v_add_u32 v22, v22, v21\n"
    "s_add_u32 s13, s40, 1088\n"
    "s_movk_i32 s15, -16\n"
    "v_mov_b32 v23, s13\n"
    "v_mad_i32_i24 v23, v3, s15, v23\n"
    "v_and_b32 v24, 1, v3\n"
    "v_sub_u32 v25, 111, v14\n"
    "v_mul_u32_u24 v26, 48, v24\n"
    "v_lshrrev_b32 v25, 1, v25\n"
    "v_lshl_add_u32 v27, v15, 1, v25\n"
    "v_sub_u32 v27, v27, v26\n"
    "v_lshl_add_u32 v27, v27, 2, s14\n"
    "v_add_u32 v32, 1, v14\n"
    "v_and_b32 v32, 3, v32\n"
    "v_xor_b32 v26, 2, v32\n"
    "v_sub_u32 v25, v26, v17\n"
    "s_add_u32 s13, s40, 42\n"
    "s_movk_i32 s15, 1216\n"
    "v_lshl_add_u32 v25, v25, 1, s13\n"
    "v_mad_u32_u24 v25, v32, s15, v25\n"
    "v_lshl_add_u32 v33, v29, 1, v24\n"
    "v_lshlrev_b32 v33, 4, v33\n"
    "v_lshrrev_b32 v34, 1, v28\n"
    "s_mov_b32 s50, 0x35f02b0\n"
    "s_mov_b32 s51, 0xbf0200\n"
    "v_lshrrev_b64 v[36:37], v33, s[50:51]\n"
    "v_sub_u16 v17, v36, v34\n"
    "v_lshl_add_u32 v17, v17, 1, s14\n"
    "s_mov_b32 s50, 0x33f0290\n"
    "s_mov_b32 s51, 0x15f001f\n"
    "v_lshrrev_b64 v[36:37], v33, s[50:51]\n"
    "v_sub_u16 v26, v36, v34\n"
    "v_lshl_add_u32 v26, v26, 1, s14\n"
    "v_lshrrev_b32 v24, 2, v3\n"
    "v_add_u32 v24, 3, v24\n"
    "v_and_b32 v24, 3, v24\n"
    "v_xor_b32 v28, 2, v24\n"
    "v_add3_u32 v28, v28, v12, 35\n"
    "v_min_u32 v28, 39, v28\n"
    "v_lshl_add_u32 v28, v28, 1, s40\n"
    "v_mad_u32_u24 v28, v24, s15, v28\n"
    "s_add_u32 s13, s40, 2320\n"
    "v_writelane_b32 v28, s13, 0\n"
    "s_add_u32 s13, s40, 2510\n"
    "v_writelane_b32 v28, s13, 1\n"
    "s_add_u32 s13, s40, 6144\n"
    "v_writelane_b32 v28, s13, 2\n"
    "v_lshrrev_b32 v29, 2, v14\n"
    "v_xor_b32 v24, v29, v15\n"
    "s_mul_i32 s13, s11, 48\n"
    "s_add_u32 s13, s13, s38\n"
    "v_lshl_add_u32 v24, v24, 3, s13\n"
    "v_add_u32 v24, v24, v21\n"
    "s_and_b32 s13, s11, 3\n"
    "s_lshl_b32 s14, s13, 7\n"
    "s_add_u32 s14, s14, %3\n"
    "s_movk_i32 s15, 8768\n"
    "v_xor_b32 v32, s13, v12\n"
    "v_lshl_add_u32 v21, v15, 5, s14\n"
    "v_lshl_add_u32 v21, v32, 3, v21\n"
    "v_mad_u32_u24 v21, v29, s15, v21\n"
    "s_lshl_b32 s13, s13, 6\n"
    "s_lshr_b32 s14, s11, 2\n"
    "s_lshl_b32 s14, s14, 8\n"
    "s_add_u32 s13, s13, s14\n"
    "s_mul_i32 s13, s13, 2560\n"
    "s_mul_i32 s14, s9, 640\n"
    "s_add_u32 s14, s14, s10\n"
    "s_lshl_b32 s14, s14, 1\n"
    "s_add_u32 s13, s13, s14\n"
    "v_mul_u32_u24 v29, 2560, v3\n"
    "v_add_u32 v29, s13, v29\n"
    "global_load_dword v12, v4, s[22:23]\n"
    "global_load_dword v14, v4, s[24:25]\n"
    "global_load_short_d16_hi v6, v5, s[16:17] offset:256\n"
    "global_load_short_d16_hi v7, v5, s[18:19] offset:256\n"
    "global_load_short_d16_hi v8, v5, s[22:23] offset:256\n"
    "global_load_short_d16_hi v9, v5, s[24:25] offset:256\n"
    "s_cmp_eq_u32 s9, 0\n"
    "s_cbranch_scc1 .Lnob%=\n"
    "global_load_dword v15, v4, s[26:27]\n"
    "global_load_dword v32, v4, s[28:29]\n"
    "global_load_short_d16_hi v10, v5, s[26:27] offset:256\n"
    "global_load_short_d16_hi v11, v5, s[28:29] offset:256\n"
    ".Lnob%=:\n"
    "s_waitcnt lgkmcnt(0)\n"
    "s_barrier\n"
    "ds_read_b128 v[36:39], v16 offset:0\n"
    "ds_read_b128 v[40:43], v16 offset:1024\n"
    "ds_read_b128 v[44:47], v16 offset:4640\n"
    "ds_read_b128 v[48:51], v16 offset:5664\n"
    "ds_read_b128 v[52:55], v16 offset:9280\n"
    "ds_read_b128 v[56:59], v16 offset:10304\n"
    "ds_read_b128 v[60:63], v16 offset:13920\n"
    "s_and_b32 s49, s12, 1\n"
    "s_lshl_b32 s49, s49, 4\n"
    "s_lshr_b32 s36, s36, s49\n"
    "s_lshl_b32 s36, s36, 16\n"
    "v_mov_b32 v4, s36\n"
    "v_mul_f32 v5, 0x3fb8aa3b, v4\n"
    "s_mov_b32 s13, 0x3fb8aa3b\n"
    "v_fma_f32 v33, v4, s13, -v5\n"
    "v_exp_f32 v5, v5\n"
    "v_fmac_f32 v33, 0x32a57060, v4\n"
    "v_mul_f32 v33, 0x3f317218, v33\n"
    "v_fma_f32 v16, v5, v33, v5\n"
    "v_cvt_pk_bf16_f32 v16, 0, v16\n"
    "s_add_u32 s15, s9, 12\n"
    "s_cmp_eq_u32 s9, 0\n"
    "s_cselect_b32 s13, 0xbfb8aa3b, s13\n"
    "v_mul_f32 v4, s13, v16\n"
    "v_mul_f32 v5, 0.15915494, v16\n"
    "s_cbranch_scc0 .Lsb%=\n"
    "s_waitcnt vmcnt(6)\n"
    "v_lshlrev_b32 v34, 16, v19\n"
    "v_and_b32 v35, 0xffff0000, v19\n"
    "v_mul_f32 v34, 0x3fb8aa3b, v34\n"
    "v_mul_f32 v35, 0x3fb8aa3b, v35\n"
    "v_exp_f32 v34, v34\n"
    "v_exp_f32 v35, v35\n"
    "s_nop 0\n"
    "v_cvt_pk_bf16_f32 v34, 0, v34\n"
    "v_cvt_pk_bf16_f32 v35, 0, v35\n"
    "s_branch .Lsj%=\n"
    ".Lsb%=:\n"
    "s_waitcnt vmcnt(10)\n"
    "v_lshlrev_b32 v34, 16, v19\n"
    "v_and_b32 v35, 0xffff0000, v19\n"
    ".Lsj%=:\n"
    "s_cmp_ge_u32 s11, 4\n"
    "s_cbranch_scc1 .Lhi%=\n"
    "s_setprio 3\n"
    "v_lshlrev_b32 v64, 16, v18\n"
    "v_mul_f32 v34, v4, v34\n"
    "v_mul_f32 v64, v5, v64\n"
    "v_exp_f32 v65, v34\n"
    "s_lshl_b32 s37, s9, 3\n"
    "s_add_u32 s37, s37, 128\n"
    "s_load_dwordx2 s[32:33], %1, s37\n"
    "s_load_dwordx2 s[34:35], %1, 144\n"
    "v_fract_f32 v66, v64\n"
    "v_and_b32 v19, 0xffff0000, v18\n"
    "v_cos_f32 v68, v66\n"
    "v_sin_f32 v69, v66\n"
    "v_mul_f32 v19, v5, v19\n"
    "v_mul_f32 v70, v65, v68\n"
    "v_mul_f32 v71, v65, v69\n"
    "v_mul_f32 v35, v4, v35\n"
    "v_pk_mul_f32 v[72:73], v[70:71], v[70:71] op_sel_hi:[0,1]\n"
    "v_fract_f32 v1, v19\n"
    "v_pk_fma_f32 v[74:75], v[70:71], v[70:71], v[72:73] op_sel:[1,1,0] op_sel_hi:[1,0,1] neg_lo:[0,1,0]\n"
    "v_exp_f32 v3, v35\n"
    "v_pk_mul_f32 v[76:77], v[74:75], v[74:75] op_sel_hi:[0,1]\n"
    "v_cos_f32 v78, v1\n"
    "v_pk_fma_f32 v[68:69], v[74:75], v[74:75], v[76:77] op_sel:[1,1,0] op_sel_hi:[1,0,1] neg_lo:[0,1,0]\n"
    "v_sin_f32 v79, v1\n"
    "v_pk_mul_f32 v[66:67], v[68:69], v[68:69] op_sel_hi:[0,1]\n"
    "v_mul_f32 v64, v3, v78\n"
    "v_pk_fma_f32 v[80:81], v[68:69], v[68:69], v[66:67] op_sel:[1,1,0] op_sel_hi:[1,0,1] neg_lo:[0,1,0]\n"
    "v_mul_f32 v65, v3, v79\n"
    "v_pk_mul_f32 v[82:83], v[80:81], v[80:81] op_sel_hi:[0,1]\n"
    "v_cvt_pk_f16_f32 v18, v80, -v81\n"
    "v_pk_fma_f32 v[72:73], v[80:81], v[80:81], v[82:83] op_sel:[1,1,0] op_sel_hi:[1,0,1] neg_lo:[0,1,0]\n"
    "ds_write_addtid_b32 v30 offset:0\n"
    "ds_write_addtid_b32 v18 offset:272\n"
    "v_cvt_pk_f16_f32 v19, v72, -v73\n"
    "v_pk_mul_f32 v[76:77], v[72:73], v[72:73] op_sel_hi:[0,1]\n"
    "v_pk_mul_f16 v1, v18, v19 op_sel_hi:[0,1]\n"
    "v_pk_fma_f32 v[84:85], v[72:73], v[72:73], v[76:77] op_sel:[1,1,0] op_sel_hi:[1,0,1] neg_lo:[0,1,0]\n"
    "v_pk_fma_f16 v86, v18, v19, v1 op_sel:[1,1,0] op_sel_hi:[1,0,1] neg_lo:[0,1,0]\n"
    "ds_write_addtid_b32 v19 offset:544\n"
    "ds_write_addtid_b32 v86 offset:816\n"
    "v_cvt_pk_f16_f32 v87, v84, -v85\n"
    "v_pk_mul_f32 v[66:67], v[84:85], v[84:85] op_sel_hi:[0,1]\n"
    "v_pk_mul_f16 v3, v18, v87 op_sel_hi:[0,1]\n"
    "v_pk_mul_f16 v78, v19, v87 op_sel_hi:[0,1]\n"
    "v_pk_fma_f16 v79, v18, v87, v3 op_sel:[1,1,0] op_sel_hi:[1,0,1] neg_lo:[0,1,0]\n"
    "ds_write_addtid_b32 v87 offset:1088\n"
    "ds_write_addtid_b32 v79 offset:1360\n"
    "v_pk_mul_f16 v80, v86, v87 op_sel_hi:[0,1]\n"
    "v_pk_fma_f16 v72, v19, v87, v78 op_sel:[1,1,0] op_sel_hi:[1,0,1] neg_lo:[0,1,0]\n"
    "v_pk_fma_f32 v[76:77], v[84:85], v[84:85], v[66:67] op_sel:[1,1,0] op_sel_hi:[1,0,1] neg_lo:[0,1,0]\n"
    "v_pk_fma_f16 v73, v86, v87, v80 op_sel:[1,1,0] op_sel_hi:[1,0,1] neg_lo:[0,1,0]\n"
    "ds_write_addtid_b32 v72 offset:1632\n"
    "ds_write_addtid_b32 v73 offset:1904\n"
    "v_cvt_pk_f16_f32 v1, v76, -v77\n"
    "v_pk_mul_f32 v[82:83], v[76:77], v[76:77] op_sel_hi:[0,1]\n"
    "v_pk_mul_f16 v81, v1, v18 op_sel_hi:[0,1]\n"
    "v_pk_mul_f16 v3, v1, v19 op_sel_hi:[0,1]\n"
    "v_pk_fma_f16 v88, v1, v18, v81 op_sel:[1,1,0] op_sel_hi:[1,0,1] neg_lo:[0,1,0]\n"
    "ds_write_addtid_b32 v1 offset:2176\n"
    "ds_write_addtid_b32 v88 offset:2448\n"
    "v_pk_mul_f16 v78, v1, v86 op_sel_hi:[0,1]\n"
    "v_pk_fma_f16 v66, v1, v19, v3 op_sel:[1,1,0] op_sel_hi:[1,0,1] neg_lo:[0,1,0]\n"
    "v_pk_fma_f16 v67, v1, v86, v78 op_sel:[1,1,0] op_sel_hi:[1,0,1] neg_lo:[0,1,0]\n"
    "v_pk_fma_f32 v[84:85], v[76:77], v[76:77], v[82:83] op_sel:[1,1,0] op_sel_hi:[1,0,1] neg_lo:[0,1,0]\n"
    "ds_write_addtid_b32 v66 offset:2720\n"
    "ds_write_addtid_b32 v67 offset:2992\n"
    "v_pk_mul_f16 v80, v1, v87 op_sel_hi:[0,1]\n"
    "v_cvt_pk_f16_f32 v89, v84, -v85\n"
    "v_pk_fma_f16 v90, v1, v87, v80 op_sel:[1,1,0] op_sel_hi:[1,0,1] neg_lo:[0,1,0]\n"
    "v_pk_mul_f16 v91, v89, v18 op_sel_hi:[0,1]\n"
    "ds_write_addtid_b32 v89 offset:4352\n"
    "ds_write_addtid_b32 v90 offset:3264\n"
    "v_pk_mul_f16 v88, v1, v79 op_sel_hi:[0,1]\n"
    "v_pk_fma_f16 v3, v89, v18, v91 op_sel:[1,1,0] op_sel_hi:[1,0,1] neg_lo:[0,1,0]\n"
    "v_pk_fma_f16 v78, v1, v79, v88 op_sel:[1,1,0] op_sel_hi:[1,0,1] neg_lo:[0,1,0]\n"
    "v_pk_mul_f16 v76, v89, v19 op_sel_hi:[0,1]\n"
    "ds_write_addtid_b32 v3 offset:4624\n"
    "ds_write_addtid_b32 v78 offset:3536\n"
    "v_pk_mul_f16 v66, v1, v72 op_sel_hi:[0,1]\n"
    "v_pk_fma_f16 v67, v89, v19, v76 op_sel:[1,1,0] op_sel_hi:[1,0,1] neg_lo:[0,1,0]\n"
    "v_pk_fma_f16 v77, v1, v72, v66 op_sel:[1,1,0] op_sel_hi:[1,0,1] neg_lo:[0,1,0]\n"
    "v_pk_mul_f16 v80, v89, v86 op_sel_hi:[0,1]\n"
    "ds_write_addtid_b32 v67 offset:4896\n"
    "ds_write_addtid_b32 v77 offset:3808\n"
    "v_pk_mul_f16 v81, v1, v73 op_sel_hi:[0,1]\n"
    "v_pk_fma_f16 v82, v89, v86, v80 op_sel:[1,1,0] op_sel_hi:[1,0,1] neg_lo:[0,1,0]\n"
    "v_pk_fma_f16 v83, v1, v73, v81 op_sel:[1,1,0] op_sel_hi:[1,0,1] neg_lo:[0,1,0]\n"
    "v_pk_mul_f16 v88, v1, v89 op_sel_hi:[0,1]\n"
    "ds_write_addtid_b32 v82 offset:5168\n"
    "ds_write_addtid_b32 v83 offset:4080\n"
    "v_pk_mul_f16 v3, v89, v87 op_sel_hi:[0,1]\n"
    "v_pk_fma_f16 v90, v1, v89, v88 op_sel:[1,1,0] op_sel_hi:[1,0,1] neg_lo:[0,1,0]\n"
    "v_pk_fma_f16 v66, v89, v87, v3 op_sel:[1,1,0] op_sel_hi:[1,0,1] neg_lo:[0,1,0]\n"
    "v_pk_mul_f16 v91, v90, v18 op_sel_hi:[0,1]\n"
    "ds_write_addtid_b32 v90 offset:6528\n"
    "ds_write_addtid_b32 v66 offset:5440\n"
    "v_pk_mul_f16 v67, v89, v79 op_sel_hi:[0,1]\n"
    "v_pk_fma_f16 v76, v90, v18, v91 op_sel:[1,1,0] op_sel_hi:[1,0,1] neg_lo:[0,1,0]\n"
    "v_pk_fma_f16 v77, v89, v79, v67 op_sel:[1,1,0] op_sel_hi:[1,0,1] neg_lo:[0,1,0]\n"
    "v_pk_mul_f16 v78, v90, v19 op_sel_hi:[0,1]\n"
    "ds_write_addtid_b32 v76 offset:6800\n"
    "ds_write_addtid_b32 v77 offset:5712\n"
    "v_pk_mul_f16 v80, v89, v72 op_sel_hi:[0,1]\n"
    "v_pk_fma_f16 v1, v90, v19, v78 op_sel:[1,1,0] op_sel_hi:[1,0,1] neg_lo:[0,1,0]\n"
    "v_pk_fma_f16 v3, v89, v72, v80 op_sel:[1,1,0] op_sel_hi:[1,0,1] neg_lo:[0,1,0]\n"
    "v_pk_mul_f16 v88, v90, v86 op_sel_hi:[0,1]\n"
    "ds_write_addtid_b32 v1 offset:7072\n"
    "ds_write_addtid_b32 v3 offset:5984\n"
    "v_pk_mul_f16 v66, v89, v73 op_sel_hi:[0,1]\n"
    "v_pk_fma_f16 v91, v90, v86, v88 op_sel:[1,1,0] op_sel_hi:[1,0,1] neg_lo:[0,1,0]\n"
    "v_pk_fma_f16 v67, v89, v73, v66 op_sel:[1,1,0] op_sel_hi:[1,0,1] neg_lo:[0,1,0]\n"
    "v_pk_mul_f16 v18, v90, v87 op_sel_hi:[0,1]\n"
    "ds_write_addtid_b32 v91 offset:7344\n"
    "ds_write_addtid_b32 v67 offset:6256\n"
    "v_pk_mul_f16 v76, v90, v79 op_sel_hi:[0,1]\n"
    "v_pk_fma_f16 v19, v90, v87, v18 op_sel:[1,1,0] op_sel_hi:[1,0,1] neg_lo:[0,1,0]\n"
    "v_pk_fma_f16 v77, v90, v79, v76 op_sel:[1,1,0] op_sel_hi:[1,0,1] neg_lo:[0,1,0]\n"
    "v_pk_mul_f16 v78, v90, v72 op_sel_hi:[0,1]\n"
    "ds_write_addtid_b32 v19 offset:7616\n"
    "ds_write_addtid_b32 v77 offset:7888\n"
    "v_pk_mul_f16 v1, v90, v73 op_sel_hi:[0,1]\n"
    "v_pk_fma_f16 v3, v90, v72, v78 op_sel:[1,1,0] op_sel_hi:[1,0,1] neg_lo:[0,1,0]\n"
    "v_pk_fma_f16 v66, v90, v73, v1 op_sel:[1,1,0] op_sel_hi:[1,0,1] neg_lo:[0,1,0]\n"
    "v_pk_mul_f32 v[80:81], v[64:65], v[64:65] op_sel_hi:[0,1]\n"
    "ds_write_addtid_b32 v3 offset:8160\n"
    "ds_write_addtid_b32 v66 offset:8432\n"
    "v_pk_fma_f32 v[82:83], v[64:65], v[64:65], v[80:81] op_sel:[1,1,0] op_sel_hi:[1,0,1] neg_lo:[0,1,0]\n"
    "v_cvt_pk_f16_f32 v18, v70, v71\n"
    "ds_read_b128 v[92:95], v31 offset:0\n"
    "v_pk_mul_f32 v[86:87], v[82:83], v[82:83] op_sel_hi:[0,1]\n"
    "v_pk_mul_f32 v[88:89], v[84:85], v[84:85] op_sel_hi:[0,1]\n"
    "ds_read_b128 v[96:99], v31 offset:32\n"
    "v_pk_fma_f32 v[72:73], v[82:83], v[82:83], v[86:87] op_sel:[1,1,0] op_sel_hi:[1,0,1] neg_lo:[0,1,0]\n"
    "v_pk_fma_f32 v[76:77], v[84:85], v[84:85], v[88:89] op_sel:[1,1,0] op_sel_hi:[1,0,1] neg_lo:[0,1,0]\n"
    "ds_read_b128 v[100:103], v31 offset:64\n"
    "v_pk_mul_f32 v[78:79], v[72:73], v[72:73] op_sel_hi:[0,1]\n"
    "v_cvt_pk_f16_f32 v19, v76, v77\n"
    "ds_read_b128 v[104:107], v31 offset:96\n"
    "v_pk_fma_f32 v[66:67], v[72:73], v[72:73], v[78:79] op_sel:[1,1,0] op_sel_hi:[1,0,1] neg_lo:[0,1,0]\n"
    "v_cvt_pk_f16_f32 v1, v74, v75\n"
    "ds_read_b128 v[108:111], v31 offset:128\n"
    "v_pk_mul_f32 v[70:71], v[66:67], v[66:67] op_sel_hi:[0,1]\n"
    "v_cvt_pk_f16_f32 v3, v66, -v67\n"
    "ds_read_b128 v[112:115], v31 offset:160\n"
    "v_pk_fma_f32 v[80:81], v[66:67], v[66:67], v[70:71] op_sel:[1,1,0] op_sel_hi:[1,0,1] neg_lo:[0,1,0]\n"
    "v_pk_mul_f32 v[84:85], v[76:77], v[76:77] op_sel_hi:[0,1]\n"
    "ds_read_b128 v[88:91], v31 offset:192\n"
    "v_cvt_pk_f16_f32 v86, v80, -v81\n"
    "v_pk_mul_f32 v[116:117], v[80:81], v[80:81] op_sel_hi:[0,1]\n"
    "ds_read_b128 v[120:123], v31 offset:224\n"
    "v_pk_mul_f16 v87, v3, v86 op_sel_hi:[0,1]\n"
    "v_pk_fma_f32 v[74:75], v[80:81], v[80:81], v[116:117] op_sel:[1,1,0] op_sel_hi:[1,0,1] neg_lo:[0,1,0]\n"
    "v_pk_fma_f16 v78, v3, v86, v87 op_sel:[1,1,0] op_sel_hi:[1,0,1] neg_lo:[0,1,0]\n"
    "v_cvt_pk_f16_f32 v79, v74, -v75\n"
    "ds_write_addtid_b32 v3 offset:272\n"
    "ds_write_addtid_b32 v86 offset:544\n"
    "v_pk_mul_f16 v66, v3, v79 op_sel_hi:[0,1]\n"
    "v_pk_mul_f16 v67, v86, v79 op_sel_hi:[0,1]\n"
    "v_pk_fma_f16 v70, v3, v79, v66 op_sel:[1,1,0] op_sel_hi:[1,0,1] neg_lo:[0,1,0]\n"
    "v_pk_fma_f16 v71, v86, v79, v67 op_sel:[1,1,0] op_sel_hi:[1,0,1] neg_lo:[0,1,0]\n"
    "ds_write_addtid_b32 v78 offset:816\n"
    "ds_write_addtid_b32 v79 offset:1088\n"
    "v_pk_mul_f32 v[118:119], v[74:75], v[74:75] op_sel_hi:[0,1]\n"
    "v_pk_mul_f16 v80, v78, v79 op_sel_hi:[0,1]\n"
    "v_pk_fma_f32 v[116:117], v[74:75], v[74:75], v[118:119] op_sel:[1,1,0] op_sel_hi:[1,0,1] neg_lo:[0,1,0]\n"
    "v_pk_fma_f16 v81, v78, v79, v80 op_sel:[1,1,0] op_sel_hi:[1,0,1] neg_lo:[0,1,0]\n"
    "ds_write_addtid_b32 v70 offset:1360\n"
    "ds_write_addtid_b32 v71 offset:1632\n"
    "v_cvt_pk_f16_f32 v87, v116, -v117\n"
    "v_pk_mul_f32 v[124:125], v[116:117], v[116:117] op_sel_hi:[0,1]\n"
    "v_pk_mul_f16 v66, v87, v3 op_sel_hi:[0,1]\n"
    "v_pk_mul_f16 v67, v87, v86 op_sel_hi:[0,1]\n"
    "ds_write_addtid_b32 v81 offset:1904\n"
    "ds_write_addtid_b32 v87 offset:2176\n"
    "v_pk_fma_f16 v126, v87, v3, v66 op_sel:[1,1,0] op_sel_hi:[1,0,1] neg_lo:[0,1,0]\n"
    "v_pk_fma_f16 v127, v87, v86, v67 op_sel:[1,1,0] op_sel_hi:[1,0,1] neg_lo:[0,1,0]\n"
    "v_pk_mul_f16 v74, v87, v78 op_sel_hi:[0,1]\n"
    "v_pk_fma_f32 v[118:119], v[116:117], v[116:117], v[124:125] op_sel:[1,1,0] op_sel_hi:[1,0,1] neg_lo:[0,1,0]\n"
    "ds_write_addtid_b32 v126 offset:2448\n"
    "ds_write_addtid_b32 v127 offset:2720\n"
    "v_pk_fma_f16 v75, v87, v78, v74 op_sel:[1,1,0] op_sel_hi:[1,0,1] neg_lo:[0,1,0]\n"
    "v_cvt_pk_f16_f32 v80, v118, -v119\n"
    "v_pk_mul_f16 v128, v87, v79 op_sel_hi:[0,1]\n"
    "v_pk_mul_f16 v129, v80, v3 op_sel_hi:[0,1]\n"
    "ds_write_addtid_b32 v75 offset:2992\n"
    "ds_write_addtid_b32 v80 offset:4352\n"
    "v_pk_fma_f16 v66, v87, v79, v128 op_sel:[1,1,0] op_sel_hi:[1,0,1] neg_lo:[0,1,0]\n"
    "v_pk_fma_f16 v67, v80, v3, v129 op_sel:[1,1,0] op_sel_hi:[1,0,1] neg_lo:[0,1,0]\n"
    "v_pk_mul_f16 v130, v87, v70 op_sel_hi:[0,1]\n"
    "v_pk_mul_f16 v116, v80, v86 op_sel_hi:[0,1]\n"
    "ds_write_addtid_b32 v66 offset:3264\n"
    "ds_write_addtid_b32 v67 offset:4624\n"
    "v_pk_fma_f16 v117, v87, v70, v130 op_sel:[1,1,0] op_sel_hi:[1,0,1] neg_lo:[0,1,0]\n"
    "v_pk_fma_f16 v74, v80, v86, v116 op_sel:[1,1,0] op_sel_hi:[1,0,1] neg_lo:[0,1,0]\n"
    "v_pk_mul_f16 v124, v87, v71 op_sel_hi:[0,1]\n"
    "v_pk_mul_f16 v125, v80, v78 op_sel_hi:[0,1]\n"
    "ds_write_addtid_b32 v117 offset:3536\n"
    "ds_write_addtid_b32 v74 offset:4896\n"
    "v_pk_fma_f16 v75, v87, v71, v124 op_sel:[1,1,0] op_sel_hi:[1,0,1] neg_lo:[0,1,0]\n"
    "v_pk_fma_f16 v126, v80, v78, v125 op_sel:[1,1,0] op_sel_hi:[1,0,1] neg_lo:[0,1,0]\n"
    "v_pk_mul_f16 v127, v87, v81 op_sel_hi:[0,1]\n"
    "v_pk_mul_f16 v128, v87, v80 op_sel_hi:[0,1]\n"
    "ds_write_addtid_b32 v75 offset:3808\n"
    "ds_write_addtid_b32 v126 offset:5168\n"
    "v_pk_fma_f16 v66, v87, v81, v127 op_sel:[1,1,0] op_sel_hi:[1,0,1] neg_lo:[0,1,0]\n"
    "v_pk_fma_f16 v67, v87, v80, v128 op_sel:[1,1,0] op_sel_hi:[1,0,1] neg_lo:[0,1,0]\n"
    "s_waitcnt vmcnt(5)\n"
    "v_lshlrev_b32 v130, 16, v12\n"
    "s_waitcnt vmcnt(4)\n"
    "v_lshlrev_b32 v131, 16, v14\n"
    "v_and_b32 v116, 0xffff0000, v12\n"
    "v_and_b32 v117, 0xffff0000, v14\n"
    "ds_write_addtid_b32 v66 offset:4080\n"
    "ds_write_addtid_b32 v67 offset:6528\n"
    "s_waitcnt vmcnt(0)\n"
    "s_cmp_eq_u32 s9, 0\n"
    "s_cbranch_scc0 .LqbA%=\n"
    "v_mul_f32 v6, 0x3fb8aa3b, v6\n"
    "v_exp_f32 v6, v6\n"
    "s_nop 0\n"
    "v_cvt_pk_bf16_f32 v6, 0, v6\n"
    "s_branch .LqjA%=\n"
    ".LqbA%=:\n"
    "v_lshlrev_b32 v124, 16, v15\n"
    "v_lshlrev_b32 v125, 16, v32\n"
    "v_and_b32 v132, 0xffff0000, v15\n"
    "v_and_b32 v133, 0xffff0000, v32\n"
    "v_pk_mul_f32 v[138:139], v[8:9], v[10:11] op_sel_hi:[0,1]\n"
    "v_pk_mul_f32 v[134:135], v[130:131], v[124:125] op_sel_hi:[0,1]\n"
    "v_pk_mul_f32 v[136:137], v[116:117], v[132:133] op_sel_hi:[0,1]\n"
    "v_pk_fma_f32 v[8:9], v[8:9], v[10:11], v[138:139] op_sel:[1,1,0] op_sel_hi:[1,0,1] neg_lo:[0,1,0]\n"
    "v_pk_fma_f32 v[130:131], v[130:131], v[124:125], v[134:135] op_sel:[1,1,0] op_sel_hi:[1,0,1] neg_lo:[0,1,0]\n"
    "v_pk_fma_f32 v[116:117], v[116:117], v[132:133], v[136:137] op_sel:[1,1,0] op_sel_hi:[1,0,1] neg_lo:[0,1,0]\n"
    ".LqjA%=:\n"
    "v_pk_mul_f16 v74, v80, v79 op_sel_hi:[0,1]\n"
    "v_pk_mul_f16 v75, v67, v3 op_sel_hi:[0,1]\n"
    "v_pk_fma_f16 v129, v80, v79, v74 op_sel:[1,1,0] op_sel_hi:[1,0,1] neg_lo:[0,1,0]\n"
    "v_pk_fma_f16 v126, v67, v3, v75 op_sel:[1,1,0] op_sel_hi:[1,0,1] neg_lo:[0,1,0]\n"
    "ds_write_addtid_b32 v129 offset:5440\n"
    "ds_write_addtid_b32 v126 offset:6800\n"
    "v_pk_mul_f16 v87, v80, v70 op_sel_hi:[0,1]\n"
    "v_pk_mul_f16 v128, v67, v86 op_sel_hi:[0,1]\n"
    "v_pk_fma_f16 v12, v80, v70, v87 op_sel:[1,1,0] op_sel_hi:[1,0,1] neg_lo:[0,1,0]\n"
    "v_pk_fma_f16 v66, v67, v86, v128 op_sel:[1,1,0] op_sel_hi:[1,0,1] neg_lo:[0,1,0]\n"
    "ds_write_addtid_b32 v12 offset:5712\n"
    "ds_write_addtid_b32 v66 offset:7072\n"
    "v_pk_mul_f16 v32, v80, v71 op_sel_hi:[0,1]\n"
    "v_pk_mul_f16 v10, v67, v78 op_sel_hi:[0,1]\n"
    "v_pk_fma_f16 v11, v80, v71, v32 op_sel:[1,1,0] op_sel_hi:[1,0,1] neg_lo:[0,1,0]\n"
    "v_pk_fma_f16 v3, v67, v78, v10 op_sel:[1,1,0] op_sel_hi:[1,0,1] neg_lo:[0,1,0]\n"
    "ds_write_addtid_b32 v11 offset:5984\n"
    "ds_write_addtid_b32 v3 offset:7344\n"
    "v_pk_mul_f16 v74, v80, v81 op_sel_hi:[0,1]\n"
    "v_pk_mul_f16 v75, v67, v79 op_sel_hi:[0,1]\n"
    "v_pk_fma_f16 v87, v80, v81, v74 op_sel:[1,1,0] op_sel_hi:[1,0,1] neg_lo:[0,1,0]\n"
    "v_pk_fma_f16 v86, v67, v79, v75 op_sel:[1,1,0] op_sel_hi:[1,0,1] neg_lo:[0,1,0]\n"
    "ds_write_addtid_b32 v87 offset:6256\n"
    "ds_write_addtid_b32 v86 offset:7616\n"
    "v_pk_mul_f16 v66, v67, v70 op_sel_hi:[0,1]\n"
    "v_mul_f32 v6, v4, v6\n"
    "v_pk_mul_f16 v32, v67, v71 op_sel_hi:[0,1]\n"
    "v_pk_fma_f16 v10, v67, v70, v66 op_sel:[1,1,0] op_sel_hi:[1,0,1] neg_lo:[0,1,0]\n"
    "v_max3_f32 v11, v34, v35, v6\n"
    "v_pk_fma_f16 v3, v67, v71, v32 op_sel:[1,1,0] op_sel_hi:[1,0,1] neg_lo:[0,1,0]\n"
    "ds_write_addtid_b32 v10 offset:7888\n"
    "ds_write_addtid_b32 v3 offset:8160\n"
    "v_max3_f32 v80, abs(v130), abs(v131), abs(v116)\n"
    "v_mul_f32 v11, 0x45000000, v11\n"
    "v_pk_mul_f16 v74, v67, v81 op_sel_hi:[0,1]\n"
    "v_max3_f32 v80, abs(v117), abs(v8), v80\n"
    "v_med3_f32 v11, v11, 0, s48\n"
    "v_pk_fma_f16 v4, v67, v81, v74 op_sel:[1,1,0] op_sel_hi:[1,0,1] neg_lo:[0,1,0]\n"
    "v_max_f32_e64 v80, abs(v9), v80\n"
    "v_ceil_f32 v11, v11\n"
    "ds_write_addtid_b32 v4 offset:8432\n"
    "v_lshrrev_b32 v80, 23, v80\n"
    "v_cvt_u32_f32 v11, v11\n"
    "ds_read_b128 v[124:127], v31 offset:0\n"
    "v_add_u32 v80, v80, v11\n"
    "v_pk_fma_f32 v[70:71], v[76:77], v[76:77], v[84:85] op_sel:[1,1,0] op_sel_hi:[1,0,1] neg_lo:[0,1,0]\n"
    "ds_read_b128 v[132:135], v31 offset:32\n"
    "v_max_u32_dpp v80, v80, v80 row_ror:8 row_mask:0xf bank_mask:0xf\n"
    "v_cvt_pk_f16_f32 v3, v70, v71\n"
    "ds_read_b128 v[136:139], v31 offset:64\n"
    "v_max_u32_dpp v80, v80, v80 row_ror:4 row_mask:0xf bank_mask:0xf\n"
    "v_cvt_pk_f16_f32 v81, v68, v69\n"
    "ds_read_b128 v[140:143], v31 offset:96\n"
    "v_max_u32_dpp v80, v80, v80 row_ror:2 row_mask:0xf bank_mask:0xf\n"
    "v_cvt_pk_f16_f32 v4, v64, v65\n"
    "ds_read_b128 v[144:147], v31 offset:128\n"
    "v_max_u32_dpp v80, v80, v80 row_ror:1 row_mask:0xf bank_mask:0xf\n"
    "v_pk_mul_f32 v[10:11], v[118:119], v[118:119] op_sel_hi:[0,1]\n"
    "v_readlane_b32 s50, v80, 0\n"
    "v_readlane_b32 s51, v80, 16\n"
    "v_readlane_b32 s52, v80, 32\n"
    "v_readlane_b32 s53, v80, 48\n"
    "ds_read_b128 v[76:79], v31 offset:160\n"
    "s_max_u32 s50, s50, s51\n"
    "s_max_u32 s52, s52, s53\n"
    "s_max_u32 s50, s50, s52\n"
    "s_max_u32 s50, s50, 14\n"
    "s_min_u32 s50, s50, 266\n"
    "s_sub_u32 s51, 267, s50\n"
    "s_lshl_b32 s51, s51, 23\n"
    "s_sub_u32 s52, s50, s15\n"
    "s_lshl_b32 s52, s52, 23\n"
    "s_mov_b32 s50, s51\n"
    "v_pk_mul_f32 v[130:131], v[130:131], s[50:51]\n"
    "ds_read_b128 v[68:71], v31 offset:192\n"
    "v_cvt_pk_f16_f32 v74, v130, v131\n"
    "v_pk_mul_f32 v[116:117], v[116:117], s[50:51]\n"
    "ds_read_b128 v[64:67], v31 offset:224\n"
    "v_pk_mul_f16 v75, v74, v18 op_sel_hi:[0,1]\n"
    "v_pk_mul_f16 v12, v74, v19 op_sel_hi:[0,1]\n"
    "v_pk_fma_f16 v14, v74, v18, v75 op_sel:[1,1,0] op_sel_hi:[1,0,1] neg_lo:[0,1,0]\n"
    "v_pk_mul_f16 v15, v74, v1 op_sel_hi:[0,1]\n"
    "ds_write_addtid_b32 v74 offset:272\n"
    "ds_write_addtid_b32 v14 offset:544\n"
    "v_pk_fma_f16 v80, v74, v19, v12 op_sel:[1,1,0] op_sel_hi:[1,0,1] neg_lo:[0,1,0]\n"
    "v_pk_fma_f16 v32, v74, v1, v15 op_sel:[1,1,0] op_sel_hi:[1,0,1] neg_lo:[0,1,0]\n"
    "v_pk_mul_f16 v34, v74, v3 op_sel_hi:[0,1]\n"
    "v_pk_mul_f16 v35, v14, v1 op_sel_hi:[0,1]\n"
    "ds_write_addtid_b32 v80 offset:1360\n"
    "ds_write_addtid_b32 v32 offset:816\n"
    "v_pk_fma_f16 v84, v74, v3, v34 op_sel:[1,1,0] op_sel_hi:[1,0,1] neg_lo:[0,1,0]\n"
    "v_pk_fma_f16 v85, v14, v1, v35 op_sel:[1,1,0] op_sel_hi:[1,0,1] neg_lo:[0,1,0]\n"
    "v_pk_mul_f16 v75, v80, v3 op_sel_hi:[0,1]\n"
    "v_pk_mul_f16 v18, v74, v81 op_sel_hi:[0,1]\n"
    "ds_write_addtid_b32 v84 offset:5712\n"
    "ds_write_addtid_b32 v85 offset:1088\n"
    "v_pk_fma_f16 v19, v80, v3, v75 op_sel:[1,1,0] op_sel_hi:[1,0,1] neg_lo:[0,1,0]\n"
    "v_pk_fma_f16 v12, v74, v81, v18 op_sel:[1,1,0] op_sel_hi:[1,0,1] neg_lo:[0,1,0]\n"
    "v_pk_mul_f16 v15, v84, v3 op_sel_hi:[0,1]\n"
    "v_pk_mul_f16 v86, v14, v81 op_sel_hi:[0,1]\n"
    "ds_write_addtid_b32 v19 offset:6256\n"
    "ds_write_addtid_b32 v12 offset:2448\n"
    "v_pk_fma_f16 v87, v84, v3, v15 op_sel:[1,1,0] op_sel_hi:[1,0,1] neg_lo:[0,1,0]\n"
    "v_pk_fma_f16 v34, v14, v81, v86 op_sel:[1,1,0] op_sel_hi:[1,0,1] neg_lo:[0,1,0]\n"
    "v_pk_mul_f16 v35, v19, v3 op_sel_hi:[0,1]\n"
    "v_pk_mul_f16 v128, v32, v81 op_sel_hi:[0,1]\n"
    "ds_write_addtid_b32 v87 offset:4624\n"
    "ds_write_addtid_b32 v34 offset:2720\n"
    "v_pk_fma_f16 v75, v19, v3, v35 op_sel:[1,1,0] op_sel_hi:[1,0,1] neg_lo:[0,1,0]\n"
    "v_pk_fma_f16 v18, v32, v81, v128 op_sel:[1,1,0] op_sel_hi:[1,0,1] neg_lo:[0,1,0]\n"
    "v_pk_mul_f16 v74, v87, v3 op_sel_hi:[0,1]\n"
    "v_pk_mul_f16 v129, v85, v81 op_sel_hi:[0,1]\n"
    "ds_write_addtid_b32 v75 offset:5168\n"
    "ds_write_addtid_b32 v18 offset:2992\n"
    "v_pk_fma_f16 v12, v87, v3, v74 op_sel:[1,1,0] op_sel_hi:[1,0,1] neg_lo:[0,1,0]\n"
    "v_pk_fma_f16 v14, v85, v81, v129 op_sel:[1,1,0] op_sel_hi:[1,0,1] neg_lo:[0,1,0]\n"
    "s_waitcnt lgkmcnt(0)\n"
    "s_load_dword s37, s[32:33], s58\n"
    "v_pk_mul_f16 v15, v75, v3 op_sel_hi:[0,1]\n"
    "ds_write_addtid_b32 v12 offset:4896\n"
    "ds_write_addtid_b32 v14 offset:3264\n"
    "v_pk_mul_f16 v34, v80, v1 op_sel_hi:[0,1]\n"
    "v_pk_fma_f16 v32, v75, v3, v15 op_sel:[1,1,0] op_sel_hi:[1,0,1] neg_lo:[0,1,0]\n"
    "v_pk_fma_f16 v35, v80, v1, v34 op_sel:[1,1,0] op_sel_hi:[1,0,1] neg_lo:[0,1,0]\n"
    "v_pk_mul_f16 v86, v12, v3 op_sel_hi:[0,1]\n"
    "ds_write_addtid_b32 v32 offset:5440\n"
    "ds_write_addtid_b32 v35 offset:1632\n"
    "v_pk_mul_f16 v18, v80, v81 op_sel_hi:[0,1]\n"
    "v_pk_fma_f16 v74, v12, v3, v86 op_sel:[1,1,0] op_sel_hi:[1,0,1] neg_lo:[0,1,0]\n"
    "v_pk_fma_f16 v85, v80, v81, v18 op_sel:[1,1,0] op_sel_hi:[1,0,1] neg_lo:[0,1,0]\n"
    "v_pk_mul_f16 v87, v35, v81 op_sel_hi:[0,1]\n"
    "ds_write_addtid_b32 v74 offset:3536\n"
    "ds_write_addtid_b32 v85 offset:1904\n"
    "v_pk_mul_f16 v14, v84, v81 op_sel_hi:[0,1]\n"
    "v_pk_fma_f16 v15, v35, v81, v87 op_sel:[1,1,0] op_sel_hi:[1,0,1] neg_lo:[0,1,0]\n"
    "v_pk_fma_f16 v1, v84, v81, v14 op_sel:[1,1,0] op_sel_hi:[1,0,1] neg_lo:[0,1,0]\n"
    "v_pk_mul_f16 v75, v19, v81 op_sel_hi:[0,1]\n"
    "ds_write_addtid_b32 v15 offset:2176\n"
    "ds_write_addtid_b32 v1 offset:5984\n"
    "v_pk_fma_f16 v32, v19, v81, v75 op_sel:[1,1,0] op_sel_hi:[1,0,1] neg_lo:[0,1,0]\n"
    "v_cvt_pk_f16_f32 v3, v116, v117\n"
    "ds_write_addtid_b32 v32 offset:6528\n"
    "v_pk_mul_f16 v18, v3, v4 op_sel_hi:[0,1]\n"
    "v_pk_fma_f32 v[128:129], v[118:119], v[118:119], v[10:11] op_sel:[1,1,0] op_sel_hi:[1,0,1] neg_lo:[0,1,0]\n"
    "ds_read_b128 v[148:151], v31 offset:272\n"
    "v_pk_fma_f16 v74, v3, v4, v18 op_sel:[1,1,0] op_sel_hi:[1,0,1] neg_lo:[0,1,0]\n"
    "v_cvt_pk_f16_f32 v80, v128, v129\n"
    "ds_read_b128 v[84:87], v31 offset:304\n"
    "v_cvt_pk_f16_f32 v130, v82, v83\n"
    "v_pk_mul_f32 v[14:15], v[128:129], v[128:129] op_sel_hi:[0,1]\n"
    "ds_read_b128 v[152:155], v31 offset:336\n"
    "global_load_dwordx4 v[156:159], v13, s[30:31] offset:0\n"
    "v_pk_mul_f16 v1, v3, v80 op_sel_hi:[0,1]\n"
    "ds_read_b128 v[160:163], v31 offset:368\n"
    "v_pk_mul_f16 v12, v3, v130 op_sel_hi:[0,1]\n"
    "v_pk_fma_f32 v[10:11], v[128:129], v[128:129], v[14:15] op_sel:[1,1,0] op_sel_hi:[1,0,1] neg_lo:[0,1,0]\n"
    "ds_read_b128 v[116:119], v31 offset:400\n"
    "v_pk_fma_f16 v4, v3, v80, v1 op_sel:[1,1,0] op_sel_hi:[1,0,1] neg_lo:[0,1,0]\n"
    "v_pk_fma_f16 v75, v3, v130, v12 op_sel:[1,1,0] op_sel_hi:[1,0,1] neg_lo:[0,1,0]\n"
    "ds_read_b128 v[164:167], v31 offset:432\n"
    "v_cvt_pk_f16_f32 v18, v10, v11\n"
    "v_pk_mul_f16 v19, v74, v130 op_sel_hi:[0,1]\n"
    "ds_read_b128 v[168:171], v31 offset:464\n"
    "v_pk_mul_f16 v32, v3, v18 op_sel_hi:[0,1]\n"
    "v_pk_fma_f16 v34, v74, v130, v19 op_sel:[1,1,0] op_sel_hi:[1,0,1] neg_lo:[0,1,0]\n"
    "ds_read_b128 v[172:175], v31 offset:496\n"
    "v_pk_fma_f16 v35, v3, v18, v32 op_sel:[1,1,0] op_sel_hi:[1,0,1] neg_lo:[0,1,0]\n"
    "v_cvt_pk_f16_f32 v14, v72, v73\n"
    "v_pk_mul_f16 v15, v4, v18 op_sel_hi:[0,1]\n"
    "v_pk_mul_f16 v1, v3, v14 op_sel_hi:[0,1]\n"
    "ds_write_addtid_b32 v3 offset:272\n"
    "ds_write_addtid_b32 v74 offset:544\n"
    "v_pk_fma_f16 v12, v4, v18, v15 op_sel:[1,1,0] op_sel_hi:[1,0,1] neg_lo:[0,1,0]\n"
    "v_pk_fma_f16 v10, v3, v14, v1 op_sel:[1,1,0] op_sel_hi:[1,0,1] neg_lo:[0,1,0]\n"
    "v_pk_mul_f16 v11, v35, v18 op_sel_hi:[0,1]\n"
    "v_pk_mul_f16 v128, v74, v14 op_sel_hi:[0,1]\n"
    "ds_write_addtid_b32 v4 offset:1360\n"
    "ds_write_addtid_b32 v75 offset:816\n"
    "v_pk_fma_f16 v19, v35, v18, v11 op_sel:[1,1,0] op_sel_hi:[1,0,1] neg_lo:[0,1,0]\n"
    "v_pk_fma_f16 v32, v74, v14, v128 op_sel:[1,1,0] op_sel_hi:[1,0,1] neg_lo:[0,1,0]\n"
    "s_waitcnt lgkmcnt(11)\n"
    "v_mfma_f32_32x32x16_f16 v[176:191], v[148:151], v[92:95], 0\n"
    "v_pk_mul_f16 v72, v12, v18 op_sel_hi:[0,1]\n"
    "ds_write_addtid_b32 v35 offset:5712\n"
    "ds_write_addtid_b32 v34 offset:1088\n"
    "v_pk_mul_f16 v15, v75, v14 op_sel_hi:[0,1]\n"
    "v_pk_fma_f16 v1, v12, v18, v72 op_sel:[1,1,0] op_sel_hi:[1,0,1] neg_lo:[0,1,0]\n"
    "v_pk_fma_f16 v3, v75, v14, v15 op_sel:[1,1,0] op_sel_hi:[1,0,1] neg_lo:[0,1,0]\n"
    "s_waitcnt lgkmcnt(12)\n"
    "v_mfma_f32_32x32x16_f16 v[176:191], v[84:87], v[96:99], v[176:191]\n"
    "ds_write_addtid_b32 v12 offset:6256\n"
    "ds_write_addtid_b32 v10 offset:2448\n"
    "v_pk_mul_f16 v11, v19, v18 op_sel_hi:[0,1]\n"
    "v_pk_mul_f16 v128, v34, v14 op_sel_hi:[0,1]\n"
    "v_pk_fma_f16 v129, v19, v18, v11 op_sel:[1,1,0] op_sel_hi:[1,0,1] neg_lo:[0,1,0]\n"
    "v_pk_fma_f16 v131, v34, v14, v128 op_sel:[1,1,0] op_sel_hi:[1,0,1] neg_lo:[0,1,0]\n"
    "ds_write_addtid_b32 v19 offset:4624\n"
    "ds_write_addtid_b32 v32 offset:2720\n"
    "s_waitcnt lgkmcnt(15)\n"
    "v_mfma_f32_32x32x16_f16 v[176:191], v[152:155], v[100:103], v[176:191]\n"
    "v_pk_mul_f16 v72, v1, v18 op_sel_hi:[0,1]\n"
    "s_setprio 2\n"
    "v_pk_mul_f16 v15, v4, v130 op_sel_hi:[0,1]\n"
    "v_pk_fma_f16 v73, v1, v18, v72 op_sel:[1,1,0] op_sel_hi:[1,0,1] neg_lo:[0,1,0]\n"
    "ds_write_addtid_b32 v1 offset:5168\n"
    "ds_write_addtid_b32 v3 offset:2992\n"
    "v_pk_fma_f16 v10, v4, v130, v15 op_sel:[1,1,0] op_sel_hi:[1,0,1] neg_lo:[0,1,0]\n"
    "s_waitcnt lgkmcnt(15)\n"
    "v_mfma_f32_32x32x16_f16 v[176:191], v[160:163], v[104:107], v[176:191]\n"
    "v_pk_mul_f16 v11, v129, v18 op_sel_hi:[0,1]\n"
    "v_pk_mul_f16 v19, v4, v14 op_sel_hi:[0,1]\n"
    "ds_write_addtid_b32 v129 offset:4896\n"
    "ds_write_addtid_b32 v131 offset:3264\n"
    "v_pk_fma_f16 v32, v129, v18, v11 op_sel:[1,1,0] op_sel_hi:[1,0,1] neg_lo:[0,1,0]\n"
    "v_pk_fma_f16 v34, v4, v14, v19 op_sel:[1,1,0] op_sel_hi:[1,0,1] neg_lo:[0,1,0]\n"
    "v_mfma_f32_32x32x16_f16 v[176:191], v[116:119], v[108:111], v[176:191]\n"
    "v_pk_mul_f16 v72, v10, v14 op_sel_hi:[0,1]\n"
    "ds_write_addtid_b32 v73 offset:5440\n"
    "ds_write_addtid_b32 v10 offset:1632\n"
    "v_pk_mul_f16 v15, v35, v14 op_sel_hi:[0,1]\n"
    "v_pk_fma_f16 v1, v10, v14, v72 op_sel:[1,1,0] op_sel_hi:[1,0,1] neg_lo:[0,1,0]\n"
    "v_pk_fma_f16 v3, v35, v14, v15 op_sel:[1,1,0] op_sel_hi:[1,0,1] neg_lo:[0,1,0]\n"
    "s_waitcnt lgkmcnt(15)\n"
    "v_mfma_f32_32x32x16_f16 v[176:191], v[164:167], v[112:115], v[176:191]\n"
    "ds_write_addtid_b32 v32 offset:3536\n"
    "ds_write_addtid_b32 v34 offset:1904\n"
    "v_pk_mul_f16 v11, v12, v14 op_sel_hi:[0,1]\n"
    "v_mul_f32 v7, v5, v7\n"
    "v_pk_fma_f16 v4, v12, v14, v11 op_sel:[1,1,0] op_sel_hi:[1,0,1] neg_lo:[0,1,0]\n"
    "v_fract_f32 v18, v7\n"
    "v_mfma_f32_32x32x16_f16 v[176:191], v[168:171], v[88:91], v[176:191]\n"
    "ds_write_addtid_b32 v1 offset:2176\n"
    "ds_write_addtid_b32 v3 offset:5984\n"
    "v_exp_f32 v19, v6\n"
    "v_cos_f32 v72, v18\n"
    "ds_write_addtid_b32 v4 offset:6528\n"
    "v_sin_f32 v73, v18\n"
    "v_mfma_f32_32x32x16_f16 v[176:191], v[172:175], v[120:123], v[176:191]\n"
    "ds_read_b128 v[80:83], v31 offset:272\n"
    "v_mul_f32 v74, v19, v72\n"
    "v_mul_f32 v75, v19, v73\n"
    "ds_read_b128 v[84:87], v31 offset:304\n"
    "global_load_dwordx4 v[88:91], v13, s[30:31] offset:1280\n"
    "v_cvt_pk_f16_f32 v1, v74, v75\n"
    "ds_read_b128 v[92:95], v31 offset:336\n"
    "v_pk_mul_f32 v[10:11], v[74:75], v[74:75] op_sel_hi:[0,1]\n"
    "v_pk_mul_f32 v[8:9], v[8:9], s[50:51]\n"
    "ds_read_b128 v[4:7], v31 offset:368\n"
    "v_pk_fma_f32 v[14:15], v[74:75], v[74:75], v[10:11] op_sel:[1,1,0] op_sel_hi:[1,0,1] neg_lo:[0,1,0]\n"
    "v_cvt_pk_f16_f32 v3, v8, v9\n"
    "v_pk_mul_f32 v[34:35], v[14:15], v[14:15] op_sel_hi:[0,1]\n"
    "ds_read_b128 v[96:99], v31 offset:400\n"
    "v_pk_fma_f32 v[18:19], v[14:15], v[14:15], v[34:35] op_sel:[1,1,0] op_sel_hi:[1,0,1] neg_lo:[0,1,0]\n"
    "v_pk_mul_f16 v12, v3, v1 op_sel_hi:[0,1]\n"
    "ds_read_b128 v[100:103], v31 offset:432\n"
    "v_pk_mul_f32 v[72:73], v[18:19], v[18:19] op_sel_hi:[0,1]\n"
    "v_pk_fma_f16 v32, v3, v1, v12 op_sel:[1,1,0] op_sel_hi:[1,0,1] neg_lo:[0,1,0]\n"
    "v_pk_fma_f32 v[104:105], v[18:19], v[18:19], v[72:73] op_sel:[1,1,0] op_sel_hi:[1,0,1] neg_lo:[0,1,0]\n"
    "ds_read_b128 v[108:111], v31 offset:464\n"
    "v_pk_mul_f32 v[106:107], v[104:105], v[104:105] op_sel_hi:[0,1]\n"
    "v_cvt_pk_f16_f32 v10, v104, -v105\n"
    "ds_read_b128 v[112:115], v31 offset:496\n"
    "v_pk_fma_f32 v[8:9], v[104:105], v[104:105], v[106:107] op_sel:[1,1,0] op_sel_hi:[1,0,1] neg_lo:[0,1,0]\n"
    "v_cvt_pk_f16_f32 v11, v14, v15\n"
    "v_cvt_pk_f16_f32 v34, v8, -v9\n"
    "v_pk_mul_f32 v[74:75], v[8:9], v[8:9] op_sel_hi:[0,1]\n"
    "ds_write_addtid_b32 v10 offset:272\n"
    "ds_write_addtid_b32 v34 offset:544\n"
    "v_pk_mul_f16 v1, v10, v34 op_sel_hi:[0,1]\n"
    "v_pk_fma_f32 v[72:73], v[8:9], v[8:9], v[74:75] op_sel:[1,1,0] op_sel_hi:[1,0,1] neg_lo:[0,1,0]\n"
    "v_pk_fma_f16 v35, v10, v34, v1 op_sel:[1,1,0] op_sel_hi:[1,0,1] neg_lo:[0,1,0]\n"
    "v_cvt_pk_f16_f32 v12, v72, -v73\n"
    "ds_write_addtid_b32 v35 offset:816\n"
    "ds_write_addtid_b32 v12 offset:1088\n"
    "v_pk_mul_f16 v104, v10, v12 op_sel_hi:[0,1]\n"
    "v_pk_mul_f16 v14, v34, v12 op_sel_hi:[0,1]\n"
    "v_pk_fma_f16 v15, v10, v12, v104 op_sel:[1,1,0] op_sel_hi:[1,0,1] neg_lo:[0,1,0]\n"
    "v_pk_fma_f16 v105, v34, v12, v14 op_sel:[1,1,0] op_sel_hi:[1,0,1] neg_lo:[0,1,0]\n"
    "ds_write_addtid_b32 v15 offset:1360\n"
    "ds_write_addtid_b32 v105 offset:1632\n"
    "v_pk_mul_f32 v[106:107], v[72:73], v[72:73] op_sel_hi:[0,1]\n"
    "v_pk_mul_f16 v8, v35, v12 op_sel_hi:[0,1]\n"
    "v_pk_fma_f32 v[74:75], v[72:73], v[72:73], v[106:107] op_sel:[1,1,0] op_sel_hi:[1,0,1] neg_lo:[0,1,0]\n"
    "v_pk_fma_f16 v1, v35, v12, v8 op_sel:[1,1,0] op_sel_hi:[1,0,1] neg_lo:[0,1,0]\n"
    "v_cvt_pk_f16_f32 v9, v74, -v75\n"
    "ds_write_addtid_b32 v1 offset:1904\n"
    "ds_write_addtid_b32 v9 offset:2176\n"
    "v_pk_mul_f16 v116, v9, v10 op_sel_hi:[0,1]\n"
    "v_pk_mul_f16 v104, v9, v34 op_sel_hi:[0,1]\n"
    "v_pk_fma_f16 v14, v9, v10, v116 op_sel:[1,1,0] op_sel_hi:[1,0,1] neg_lo:[0,1,0]\n"
    "v_pk_fma_f16 v117, v9, v34, v104 op_sel:[1,1,0] op_sel_hi:[1,0,1] neg_lo:[0,1,0]\n"
    "ds_write_addtid_b32 v14 offset:2448\n"
    "ds_write_addtid_b32 v117 offset:2720\n"
    "v_pk_mul_f32 v[118:119], v[74:75], v[74:75] op_sel_hi:[0,1]\n"
    "v_pk_mul_f16 v72, v9, v35 op_sel_hi:[0,1]\n"
    "v_pk_fma_f32 v[106:107], v[74:75], v[74:75], v[118:119] op_sel:[1,1,0] op_sel_hi:[1,0,1] neg_lo:[0,1,0]\n"
    "v_pk_fma_f16 v8, v9, v35, v72 op_sel:[1,1,0] op_sel_hi:[1,0,1] neg_lo:[0,1,0]\n"
    "v_cvt_pk_f16_f32 v73, v106, -v107\n"
    "ds_write_addtid_b32 v8 offset:2992\n"
    "ds_write_addtid_b32 v73 offset:4352\n"
    "v_pk_mul_f16 v120, v9, v12 op_sel_hi:[0,1]\n"
    "v_pk_mul_f16 v121, v73, v10 op_sel_hi:[0,1]\n"
    "v_pk_fma_f16 v104, v9, v12, v120 op_sel:[1,1,0] op_sel_hi:[1,0,1] neg_lo:[0,1,0]\n"
    "v_pk_fma_f16 v14, v73, v10, v121 op_sel:[1,1,0] op_sel_hi:[1,0,1] neg_lo:[0,1,0]\n"
    "ds_write_addtid_b32 v104 offset:3264\n"
    "ds_write_addtid_b32 v14 offset:4624\n"
    "s_waitcnt lgkmcnt(15)\n"
    "v_mfma_f32_32x32x16_f16 v[176:191], v[80:83], v[124:127], v[176:191]\n"
    "v_pk_mul_f16 v74, v9, v15 op_sel_hi:[0,1]\n"
    "v_pk_mul_f16 v72, v73, v34 op_sel_hi:[0,1]\n"
    "v_pk_fma_f16 v75, v9, v15, v74 op_sel:[1,1,0] op_sel_hi:[1,0,1] neg_lo:[0,1,0]\n"
    "v_pk_fma_f16 v8, v73, v34, v72 op_sel:[1,1,0] op_sel_hi:[1,0,1] neg_lo:[0,1,0]\n"
    "ds_write_addtid_b32 v75 offset:3536\n"
    "ds_write_addtid_b32 v8 offset:4896\n"
    "v_mfma_f32_32x32x16_f16 v[176:191], v[84:87], v[132:135], v[176:191]\n"
    "v_pk_mul_f16 v116, v9, v105 op_sel_hi:[0,1]\n"
    "v_pk_mul_f16 v117, v73, v35 op_sel_hi:[0,1]\n"
    "v_pk_fma_f16 v104, v9, v105, v116 op_sel:[1,1,0] op_sel_hi:[1,0,1] neg_lo:[0,1,0]\n"
    "v_pk_fma_f16 v14, v73, v35, v117 op_sel:[1,1,0] op_sel_hi:[1,0,1] neg_lo:[0,1,0]\n"
    "ds_write_addtid_b32 v104 offset:3808\n"
    "ds_write_addtid_b32 v14 offset:5168\n"
    "v_mfma_f32_32x32x16_f16 v[176:191], v[92:95], v[136:139], v[176:191]\n"
    "v_pk_mul_f16 v74, v9, v1 op_sel_hi:[0,1]\n"
    "v_pk_mul_f16 v72, v9, v73 op_sel_hi:[0,1]\n"
    "v_pk_fma_f16 v75, v9, v1, v74 op_sel:[1,1,0] op_sel_hi:[1,0,1] neg_lo:[0,1,0]\n"
    "v_pk_fma_f16 v8, v9, v73, v72 op_sel:[1,1,0] op_sel_hi:[1,0,1] neg_lo:[0,1,0]\n"
    "ds_write_addtid_b32 v75 offset:4080\n"
    "ds_write_addtid_b32 v8 offset:6528\n"
    "v_mfma_f32_32x32x16_f16 v[176:191], v[4:7], v[140:143], v[176:191]\n"
    "v_pk_mul_f16 v80, v73, v12 op_sel_hi:[0,1]\n"
    "v_pk_mul_f16 v81, v8, v10 op_sel_hi:[0,1]\n"
    "v_pk_fma_f16 v104, v73, v12, v80 op_sel:[1,1,0] op_sel_hi:[1,0,1] neg_lo:[0,1,0]\n"
    "v_pk_fma_f16 v14, v8, v10, v81 op_sel:[1,1,0] op_sel_hi:[1,0,1] neg_lo:[0,1,0]\n"
    "ds_write_addtid_b32 v104 offset:5440\n"
    "ds_write_addtid_b32 v14 offset:6800\n"
    "v_mfma_f32_32x32x16_f16 v[176:191], v[96:99], v[144:147], v[176:191]\n"
    "v_pk_mul_f16 v74, v73, v15 op_sel_hi:[0,1]\n"
    "global_load_dwordx4 v[84:87], v13, s[30:31] offset:2560\n"
    "v_pk_mul_f16 v9, v8, v34 op_sel_hi:[0,1]\n"
    "v_pk_fma_f16 v72, v73, v15, v74 op_sel:[1,1,0] op_sel_hi:[1,0,1] neg_lo:[0,1,0]\n"
    "v_pk_fma_f16 v75, v8, v34, v9 op_sel:[1,1,0] op_sel_hi:[1,0,1] neg_lo:[0,1,0]\n"
    "ds_write_addtid_b32 v72 offset:5712\n"
    "ds_write_addtid_b32 v75 offset:7072\n"
    "v_mfma_f32_32x32x16_f16 v[176:191], v[100:103], v[76:79], v[176:191]\n"
    "v_pk_mul_f16 v10, v73, v105 op_sel_hi:[0,1]\n"
    "v_pk_mul_f16 v104, v8, v35 op_sel_hi:[0,1]\n"
    "v_pk_fma_f16 v14, v73, v105, v10 op_sel:[1,1,0] op_sel_hi:[1,0,1] neg_lo:[0,1,0]\n"
    "v_pk_fma_f16 v4, v8, v35, v104 op_sel:[1,1,0] op_sel_hi:[1,0,1] neg_lo:[0,1,0]\n"
    "ds_write_addtid_b32 v14 offset:5984\n"
    "ds_write_addtid_b32 v4 offset:7344\n"
    "v_mfma_f32_32x32x16_f16 v[176:191], v[108:111], v[68:71], v[176:191]\n"
    "v_pk_mul_f16 v74, v73, v1 op_sel_hi:[0,1]\n"
    "v_pk_mul_f16 v9, v8, v12 op_sel_hi:[0,1]\n"
    "v_pk_fma_f16 v34, v73, v1, v74 op_sel:[1,1,0] op_sel_hi:[1,0,1] neg_lo:[0,1,0]\n"
    "v_pk_fma_f16 v5, v8, v12, v9 op_sel:[1,1,0] op_sel_hi:[1,0,1] neg_lo:[0,1,0]\n"
    "ds_write_addtid_b32 v34 offset:6256\n"
    "ds_write_addtid_b32 v5 offset:7616\n"
    "s_waitcnt lgkmcnt(15)\n"
    "v_mfma_f32_32x32x16_f16 v[176:191], v[112:115], v[64:67], v[176:191]\n"
    "v_pk_mul_f16 v10, v8, v15 op_sel_hi:[0,1]\n"
    "v_pk_mul_f16 v104, v8, v105 op_sel_hi:[0,1]\n"
    "v_pk_fma_f16 v14, v8, v15, v10 op_sel:[1,1,0] op_sel_hi:[1,0,1] neg_lo:[0,1,0]\n"
    "v_pk_fma_f16 v35, v8, v105, v104 op_sel:[1,1,0] op_sel_hi:[1,0,1] neg_lo:[0,1,0]\n"
    "ds_write_addtid_b32 v14 offset:7888\n"
    "ds_write_addtid_b32 v35 offset:8160\n"
    "v_pk_mul_f16 v4, v8, v1 op_sel_hi:[0,1]\n"
    "v_pk_mul_f32 v[6:7], v[106:107], v[106:107] op_sel_hi:[0,1]\n"
    "v_pk_fma_f16 v9, v8, v1, v4 op_sel:[1,1,0] op_sel_hi:[1,0,1] neg_lo:[0,1,0]\n"
    "v_pk_fma_f32 v[68:69], v[106:107], v[106:107], v[6:7] op_sel:[1,1,0] op_sel_hi:[1,0,1] neg_lo:[0,1,0]\n"
    "ds_write_addtid_b32 v9 offset:8432\n"
    "v_cvt_pk_f16_f32 v34, v68, v69\n"
    "v_pk_mul_f32 v[70:71], v[68:69], v[68:69] op_sel_hi:[0,1]\n"
    "ds_read_b128 v[64:67], v31 offset:0\n"
    "v_pk_mul_f16 v10, v3, v34 op_sel_hi:[0,1]\n"
    "v_pk_mul_f16 v12, v3, v11 op_sel_hi:[0,1]\n"
    "ds_read_b128 v[72:75], v31 offset:32\n"
    "v_pk_fma_f32 v[14:15], v[68:69], v[68:69], v[70:71] op_sel:[1,1,0] op_sel_hi:[1,0,1] neg_lo:[0,1,0]\n"
    "v_pk_fma_f16 v35, v3, v34, v10 op_sel:[1,1,0] op_sel_hi:[1,0,1] neg_lo:[0,1,0]\n"
    "ds_read_b128 v[76:79], v31 offset:64\n"
    "v_pk_fma_f16 v1, v3, v11, v12 op_sel:[1,1,0] op_sel_hi:[1,0,1] neg_lo:[0,1,0]\n"
    "v_cvt_pk_f16_f32 v8, v14, v15\n"
    "ds_read_b128 v[4:7], v31 offset:96\n"
    "v_pk_mul_f16 v9, v3, v8 op_sel_hi:[0,1]\n"
    "v_pk_mul_f16 v80, v32, v11 op_sel_hi:[0,1]\n"
    "ds_read_b128 v[92:95], v31 offset:128\n"
    "v_pk_fma_f16 v81, v3, v8, v9 op_sel:[1,1,0] op_sel_hi:[1,0,1] neg_lo:[0,1,0]\n"
    "v_pk_fma_f16 v82, v32, v11, v80 op_sel:[1,1,0] op_sel_hi:[1,0,1] neg_lo:[0,1,0]\n"
    "ds_read_b128 v[96:99], v31 offset:160\n"
    "v_cvt_pk_f16_f32 v83, v18, v19\n"
    "v_pk_mul_f16 v34, v35, v8 op_sel_hi:[0,1]\n"
    "ds_read_b128 v[68:71], v31 offset:192\n"
    "v_pk_mul_f16 v10, v3, v83 op_sel_hi:[0,1]\n"
    "v_pk_fma_f16 v12, v35, v8, v34 op_sel:[1,1,0] op_sel_hi:[1,0,1] neg_lo:[0,1,0]\n"
    "ds_read_b128 v[100:103], v31 offset:224\n"
    "v_pk_fma_f16 v14, v3, v83, v10 op_sel:[1,1,0] op_sel_hi:[1,0,1] neg_lo:[0,1,0]\n"
    "v_pk_mul_f16 v15, v81, v8 op_sel_hi:[0,1]\n"
    "global_load_dwordx4 v[104:107], v13, s[30:31] offset:3840\n"
    "v_pk_mul_f16 v9, v32, v83 op_sel_hi:[0,1]\n"
    "ds_write_addtid_b32 v3 offset:272\n"
    "ds_write_addtid_b32 v32 offset:544\n"
    "v_pk_fma_f16 v80, v81, v8, v15 op_sel:[1,1,0] op_sel_hi:[1,0,1] neg_lo:[0,1,0]\n"
    "v_pk_fma_f16 v18, v32, v83, v9 op_sel:[1,1,0] op_sel_hi:[1,0,1] neg_lo:[0,1,0]\n"
    "v_pk_mul_f16 v19, v12, v8 op_sel_hi:[0,1]\n"
    "v_pk_mul_f16 v108, v1, v83 op_sel_hi:[0,1]\n"
    "ds_write_addtid_b32 v35 offset:1360\n"
    "s_setprio 1\n"
    "ds_write_addtid_b32 v1 offset:816\n"
    "v_pk_fma_f16 v10, v12, v8, v19 op_sel:[1,1,0] op_sel_hi:[1,0,1] neg_lo:[0,1,0]\n"
    "v_pk_fma_f16 v13, v1, v83, v108 op_sel:[1,1,0] op_sel_hi:[1,0,1] neg_lo:[0,1,0]\n"
    "v_pk_mul_f16 v34, v80, v8 op_sel_hi:[0,1]\n"
    "v_pk_mul_f16 v3, v82, v83 op_sel_hi:[0,1]\n"
    "ds_write_addtid_b32 v81 offset:5712\n"
    "ds_write_addtid_b32 v82 offset:1088\n"
    "v_pk_fma_f16 v9, v80, v8, v34 op_sel:[1,1,0] op_sel_hi:[1,0,1] neg_lo:[0,1,0]\n"
    "v_pk_fma_f16 v15, v82, v83, v3 op_sel:[1,1,0] op_sel_hi:[1,0,1] neg_lo:[0,1,0]\n"
    "s_waitcnt lgkmcnt(0)\n"
    "s_lshr_b32 s37, s37, s49\n"
    "s_and_b32 s37, s37, 0xffff\n"
    "v_pk_mul_f16 v32, v10, v8 op_sel_hi:[0,1]\n"
    "ds_write_addtid_b32 v12 offset:6256\n"
    "ds_write_addtid_b32 v14 offset:2448\n"
    "v_pk_mul_f16 v19, v35, v11 op_sel_hi:[0,1]\n"
    "v_pk_fma_f16 v1, v10, v8, v32 op_sel:[1,1,0] op_sel_hi:[1,0,1] neg_lo:[0,1,0]\n"
    "v_pk_fma_f16 v108, v35, v11, v19 op_sel:[1,1,0] op_sel_hi:[1,0,1] neg_lo:[0,1,0]\n"
    "v_pk_mul_f16 v109, v9, v8 op_sel_hi:[0,1]\n"
    "ds_write_addtid_b32 v80 offset:4624\n"
    "ds_write_addtid_b32 v18 offset:2720\n"
    "v_pk_mul_f16 v34, v35, v83 op_sel_hi:[0,1]\n"
    "v_pk_fma_f16 v3, v9, v8, v109 op_sel:[1,1,0] op_sel_hi:[1,0,1] neg_lo:[0,1,0]\n"
    "v_mul_u32_u24 v60, s37, v60\n"
    "v_mul_u32_u24 v61, s37, v61\n"
    "ds_write_addtid_b32 v10 offset:5168\n"
    "ds_write_addtid_b32 v13 offset:2992\n"
    "v_mul_u32_u24 v62, s37, v62\n"
    "v_mul_u32_u24 v63, s37, v63\n"
    "v_pk_fma_f16 v32, v35, v83, v34 op_sel:[1,1,0] op_sel_hi:[1,0,1] neg_lo:[0,1,0]\n"
    "v_pk_mul_f16 v14, v108, v83 op_sel_hi:[0,1]\n"
    "ds_write_addtid_b32 v9 offset:4896\n"
    "ds_write_addtid_b32 v15 offset:3264\n"
    "v_pk_mul_f16 v18, v81, v83 op_sel_hi:[0,1]\n"
    "v_pk_fma_f16 v19, v108, v83, v14 op_sel:[1,1,0] op_sel_hi:[1,0,1] neg_lo:[0,1,0]\n"
    "v_pk_fma_f16 v80, v81, v83, v18 op_sel:[1,1,0] op_sel_hi:[1,0,1] neg_lo:[0,1,0]\n"
    "v_pk_mul_f16 v82, v12, v83 op_sel_hi:[0,1]\n"
    "ds_write_addtid_b32 v1 offset:5440\n"
    "ds_write_addtid_b32 v108 offset:1632\n"
    "v_pk_fma_f16 v13, v12, v83, v82 op_sel:[1,1,0] op_sel_hi:[1,0,1] neg_lo:[0,1,0]\n"
    "s_waitcnt vmcnt(2)\n"
    "v_perm_b32 v10, v88, v156, s41\n"
    "s_waitcnt vmcnt(0)\n"
    "v_perm_b32 v11, v104, v84, s41\n"
    "v_perm_b32 v34, v88, v156, s42\n"
    "ds_write_addtid_b32 v3 offset:3536\n"
    "ds_write_addtid_b32 v32 offset:1904\n"
    "v_perm_b32 v35, v104, v84, s42\n"
    "v_perm_b32 v8, v89, v157, s41\n"
    "v_perm_b32 v9, v105, v85, s41\n"
    "v_perm_b32 v14, v89, v157, s42\n"
    "ds_write_addtid_b32 v19 offset:2176\n"
    "ds_write_addtid_b32 v80 offset:5984\n"
    "v_perm_b32 v15, v105, v85, s42\n"
    "v_perm_b32 v82, v90, v158, s41\n"
    "ds_write_addtid_b32 v13 offset:6528\n"
    "v_perm_b32 v83, v106, v86, s41\n"
    "v_perm_b32 v108, v90, v158, s42\n"
    "ds_read_b128 v[112:115], v31 offset:272\n"
    "v_perm_b32 v109, v106, v86, s42\n"
    "v_perm_b32 v110, v91, v159, s41\n"
    "ds_read_b128 v[116:119], v31 offset:304\n"
    "v_perm_b32 v111, v107, v87, s41\n"
    "v_perm_b32 v18, v91, v159, s42\n"
    "ds_read_b128 v[120:123], v31 offset:336\n"
    "v_perm_b32 v19, v107, v87, s42\n"
    "v_mul_f32 v33, s52, v16\n"
    "ds_read_b128 v[124:127], v31 offset:368\n"
    "ds_read_b128 v[128:131], v31 offset:400\n"
    "ds_read_b128 v[132:135], v31 offset:432\n"
    "ds_read_b128 v[136:139], v31 offset:464\n"
    "ds_read_b128 v[140:143], v31 offset:496\n"
    "ds_write_b64 v20, v[10:11] offset:0\n"
    "ds_write_b64 v20, v[34:35] offset:4640\n"
    "ds_write_b64 v20, v[8:9] offset:9280\n"
    "ds_write_b64 v20, v[14:15] offset:13920\n"
    "ds_write_b64 v20, v[82:83] offset:18560\n"
    "ds_write_b64 v20, v[108:109] offset:23200\n"
    "ds_write_b64 v20, v[110:111] offset:27840\n"
    "ds_write_b64 v20, v[18:19] offset:32480\n"
    "ds_write_b32 v0, v30\n"
    "ds_read_b32 v1, v2\n"
    "s_waitcnt lgkmcnt(15)\n"
    "v_mfma_f32_32x32x16_f16 v[176:191], v[112:115], v[64:67], v[176:191]\n"
    "v_mfma_f32_32x32x16_f16 v[176:191], v[116:119], v[72:75], v[176:191]\n"
    "v_mfma_f32_32x32x16_f16 v[176:191], v[120:123], v[76:79], v[176:191]\n"
    "s_waitcnt lgkmcnt(0)\n"
    "v_cmp_ne_u32 vcc, 0, v1\n"
    "s_cmp_eq_u64 vcc, -1\n"
    "s_cbranch_scc1 .Lok1A%=\n"
    ".Lsp1A%=:\n"
    "s_sleep 1\n"
    "ds_read_b32 v1, v2\n"
    "s_waitcnt lgkmcnt(0)\n"
    "v_cmp_ne_u32 vcc, 0, v1\n"
    "s_cmp_eq_u64 vcc, -1\n"
    "s_cbranch_scc0 .Lsp1A%=\n"
    ".Lok1A%=:\n"
    "s_setprio 0\n"
    "ds_read_b128 v[8:11], v22 offset:512\n"
    "ds_read_b128 v[12:15], v22 offset:496\n"
    "v_mfma_f32_32x32x16_f16 v[176:191], v[124:127], v[4:7], v[176:191]\n"
    "ds_read_b128 v[80:83], v22 offset:1024\n"
    "ds_read_b128 v[84:87], v22 offset:1008\n"
    "v_mfma_f32_32x32x16_f16 v[176:191], v[128:131], v[92:95], v[176:191]\n"
    "v_mfma_f32_32x32x16_f16 v[176:191], v[132:135], v[96:99], v[176:191]\n"
    "v_mfma_f32_32x32x16_f16 v[176:191], v[136:139], v[68:71], v[176:191]\n"
    "v_mfma_f32_32x32x16_f16 v[176:191], v[140:143], v[100:103], v[176:191]\n"
    "ds_read_b128 v[64:67], v22 offset:64\n"
    "ds_read_b128 v[0:3], v22 offset:128\n"
    "s_waitcnt lgkmcnt(5)\n"
    "v_mfma_f32_16x16x32_bf16 v[72:75], v[8:11], v[36:39], 0\n"
    "ds_read_b128 v[76:79], v22 offset:192\n"
    "ds_read_b128 v[88:91], v22 offset:256\n"
    "v_mfma_f32_16x16x32_bf16 v[4:7], v[8:11], v[40:43], 0\n"
    "ds_read_b128 v[104:107], v22 offset:320\n"
    "v_mfma_f32_16x16x32_bf16 v[108:111], v[8:11], v[44:47], 0\n"
    "s_waitcnt lgkmcnt(7)\n"
    "v_mfma_f32_16x16x32_bf16 v[92:95], v[12:15], v[40:43], 0\n"
    "v_mfma_f32_16x16x32_bf16 v[96:99], v[12:15], v[48:51], 0\n"
    "v_mfma_f32_16x16x32_bf16 v[68:71], v[8:11], v[52:55], 0\n"
    "v_cvt_pk_bf16_f32 v100, v72, v73\n"
    "v_mul_f32 v20, v33, v184\n"
    "s_waitcnt lgkmcnt(6)\n"
    "v_mfma_f32_16x16x32_bf16 v[112:115], v[80:83], v[36:39], 0\n"
    "v_cvt_pk_bf16_f32 v101, v74, v75\n"
    "v_readlane_b32 s53, v20, 32\n"
    "v_mfma_f32_16x16x32_bf16 v[116:119], v[80:83], v[40:43], 0\n"
    "v_mul_f32 v30, v33, v176\n"
    "v_cvt_pk_bf16_f32 v102, v4, v5\n"
    "v_mfma_f32_16x16x32_bf16 v[120:123], v[80:83], v[44:47], 0\n"
    "v_cvt_pk_bf16_f32 v103, v6, v7\n"
    "v_mov_b32 v31, s53\n"
    "s_waitcnt lgkmcnt(5)\n"
    "v_mfma_f32_16x16x32_bf16 v[8:11], v[84:87], v[40:43], 0\n"
    "v_mul_f32 v16, v33, v179\n"
    "v_readlane_b32 s56, v30, 0\n"
    "v_mfma_f32_16x16x32_bf16 v[12:15], v[84:87], v[48:51], 0\n"
    "v_mov_b32 v18, s53\n"
    "v_cvt_pk_bf16_f32 v72, v108, v109\n"
    "v_mfma_f32_16x16x32_bf16 v[124:127], v[80:83], v[52:55], 0\n"
    "v_cvt_pk_bf16_f32 v73, v110, v111\n"
    "v_mul_f32 v19, v33, v180\n"
    "v_mul_f32 v32, v33, v182\n"
    "v_mul_f32 v34, v33, v183\n"
    "v_readlane_b32 s57, v16, 63\n"
    "v_mov_b32_dpp v31, v30 wave_shl:1 row_mask:0xf bank_mask:0xf\n"
    "v_writelane_b32 v18, s56, 0\n"
    "v_readlane_b32 s55, v19, 32\n"
    "v_cndmask_b32_e64 v35, v32, v16, s[44:45]\n"
    "v_cndmask_b32_e64 v4, v34, v31, s[44:45]\n"
    "v_cvt_pk_bf16_f32 v74, v92, v93\n"
    "v_cvt_pk_bf16_f32 v75, v94, v95\n"
    "v_mul_f32 v5, v33, v177\n"
    "v_mul_f32 v6, v33, v178\n"
    "v_mul_f32 v7, v33, v181\n"
    "v_writelane_b32 v18, s57, 1\n"
    "v_cndmask_b32_e64 v80, v5, v30, s[44:45]\n"
    "v_cndmask_b32_e64 v81, v6, v5, s[44:45]\n"
    "v_cndmask_b32_e64 v82, v16, v6, s[44:45]\n"
    "v_cndmask_b32_e64 v83, v19, v6, s[44:45]\n"
    "v_cndmask_b32_e64 v32, v7, v6, s[44:45]\n"
    "v_mov_b32_dpp v34, v4 wave_shr:1 row_mask:0xf bank_mask:0xf bound_ctrl:0\n"
    "v_mov_b32_dpp v84, v35 wave_shr:1 row_mask:0xf bank_mask:0xf bound_ctrl:0\n"
    "v_writelane_b32 v18, s55, 2\n"
    "v_cvt_pk_bf16_f32 v92, v96, v97\n"
    "v_cvt_pk_bf16_f32 v93, v98, v99\n"
    "v_cvt_pk_bf16_f32 v108, v4, v35\n"
    "v_cvt_pk_bf16_f32 v109, v32, v83\n"
    "v_cvt_pk_bf16_f32 v110, v82, v81\n"
    "v_cvt_pk_bf16_f32 v111, v80, v30\n"
    "v_cvt_pk_bf16_f32 v128, v31, v4\n"
    "v_cvt_pk_bf16_f32 v129, v35, v32\n"
    "v_cvt_pk_bf16_f32 v130, v83, v82\n"
    "v_cvt_pk_bf16_f32 v131, v81, v80\n"
    "v_cvt_pk_bf16_f32 v132, v32, v83\n"
    "v_cvt_pk_bf16_f32 v133, v82, v81\n"
    "v_cvt_pk_bf16_f32 v134, v80, v30\n"
    "v_cvt_pk_bf16_f32 v135, v34, v84\n"
    "v_cvt_pk_bf16_f32 v96, v35, v32\n"
    "v_cvt_pk_bf16_f32 v97, v83, v82\n"
    "v_cvt_pk_bf16_f32 v98, v81, v80\n"
    "v_cvt_pk_bf16_f32 v99, v30, v34\n"
    "v_cvt_pk_bf16_f32 v18, v18, v18\n"
    "v_cvt_pk_bf16_f32 v94, v68, v69\n"
    "v_cvt_pk_bf16_f32 v95, v70, v71\n"
    "ds_write_b128 v23, v[108:111] offset:0\n"
    "ds_write_b128 v23, v[128:131] offset:1216\n"
    "ds_write_b128 v23, v[132:135] offset:2432\n"
    "ds_write_b128 v23, v[96:99] offset:3648\n"
    "s_mov_b64 exec, 0xfffff\n"
    "ds_write_b16 v28, v18\n"
    "s_mov_b64 exec, -1\n"
    "v_cvt_pk_bf16_f32 v4, v112, v113\n"
    "v_cvt_pk_bf16_f32 v5, v114, v115\n"
    "ds_read_b64 v[80:81], v25 offset:576\n"
    "ds_read_b64 v[82:83], v25 offset:584\n"
    "v_cvt_pk_bf16_f32 v6, v116, v117\n"
    "v_cvt_pk_bf16_f32 v7, v118, v119\n"
    "ds_read_b64 v[68:69], v25 offset:64\n"
    "ds_read_b64 v[70:71], v25 offset:72\n"
    "v_cvt_pk_bf16_f32 v84, v120, v121\n"
    "v_cvt_pk_bf16_f32 v85, v122, v123\n"
    "v_cvt_pk_bf16_f32 v86, v8, v9\n"
    "v_cvt_pk_bf16_f32 v87, v10, v11\n"
    "ds_read_b64 v[96:97], v25 offset:640\n"
    "ds_read_b64 v[98:99], v25 offset:648\n"
    "v_cvt_pk_bf16_f32 v108, v12, v13\n"
    "v_cvt_pk_bf16_f32 v109, v14, v15\n"
    "ds_read_b64 v[112:113], v25 offset:128\n"
    "ds_read_b64 v[114:115], v25 offset:136\n"
    "v_cvt_pk_bf16_f32 v110, v124, v125\n"
    "v_cvt_pk_bf16_f32 v111, v126, v127\n"
    "ds_read_b64 v[116:117], v25 offset:704\n"
    "ds_read_b64 v[118:119], v25 offset:712\n"
    "v_mul_f32 v23, v33, v185\n"
    "v_mul_f32 v16, v33, v186\n"
    "ds_read_b64 v[8:9], v25 offset:192\n"
    "ds_read_b64 v[10:11], v25 offset:200\n"
    "v_mul_f32 v18, v33, v187\n"
    "v_cndmask_b32_e64 v19, v20, 0, s[46:47]\n"
    "ds_read_b64 v[12:13], v25 offset:768\n"
    "ds_read_b64 v[14:15], v25 offset:776\n"
    "v_cvt_pk_bf16_f32 v28, v19, v23\n"
    "v_cvt_pk_bf16_f32 v30, v16, v18\n"
    "ds_read_b64 v[120:121], v25 offset:256\n"
    "s_waitcnt lgkmcnt(13)\n"
    "v_mfma_f32_16x16x32_bf16 v[124:127], v[80:83], v[64:67], 0\n"
    "ds_read_b64 v[122:123], v25 offset:264\n"
    "v_add_u32 v31, 640, v27\n"
    "ds_read_b64 v[128:129], v25 offset:832\n"
    "s_waitcnt lgkmcnt(13)\n"
    "v_mfma_f32_16x16x32_bf16 v[132:135], v[68:71], v[64:67], 0\n"
    "ds_read_b64 v[130:131], v25 offset:840\n"
    "ds_read_b128 v[32:35], v22 offset:384\n"
    "ds_read_b64 v[136:137], v25 offset:320\n"
    "ds_read_b64 v[138:139], v25 offset:328\n"
    "v_readlane_b32 s54, v20, 0\n"
    "s_waitcnt lgkmcnt(15)\n"
    "v_mfma_f32_16x16x32_bf16 v[124:127], v[96:99], v[0:3], v[124:127]\n"
    "ds_read_b64 v[140:141], v25 offset:896\n"
    "ds_read_b64 v[142:143], v25 offset:904\n"
    "v_cvt_pk_bf16_f32 v23, s54, 0\n"
    "s_waitcnt lgkmcnt(15)\n"
    "v_mfma_f32_16x16x32_bf16 v[132:135], v[112:115], v[0:3], v[132:135]\n"
    "ds_read_b64 v[144:145], v25 offset:384\n"
    "ds_read_b64 v[146:147], v25 offset:392\n"
    "ds_read_b128 v[64:67], v22 offset:448\n"
    "s_waitcnt lgkmcnt(15)\n"
    "v_mfma_f32_16x16x32_bf16 v[124:127], v[116:119], v[76:79], v[124:127]\n"
    "ds_read_b64 v[148:149], v25 offset:960\n"
    "ds_read_b64 v[150:151], v25 offset:968\n"
    "v_mul_u32_u24 v56, v23, v56\n"
    "s_waitcnt lgkmcnt(15)\n"
    "v_mfma_f32_16x16x32_bf16 v[132:135], v[8:11], v[76:79], v[132:135]\n"
    "ds_read_b64 v[152:153], v25 offset:448\n"
    "ds_read_b64 v[154:155], v25 offset:456\n"
    "ds_read_b128 v[156:159], v22 offset:512\n"
    "s_waitcnt lgkmcnt(15)\n"
    "v_mfma_f32_16x16x32_bf16 v[124:127], v[12:15], v[88:91], v[124:127]\n"
    "ds_read_b64 v[0:1], v25 offset:1024\n"
    "ds_read_b64 v[2:3], v25 offset:1032\n"
    "v_mul_u32_u24 v57, v23, v57\n"
    "v_mul_u32_u24 v58, v23, v58\n"
    "v_mfma_f32_16x16x32_bf16 v[132:135], v[120:123], v[88:91], v[132:135]\n"
    "ds_read_b64 v[160:161], v25 offset:512\n"
    "ds_read_b64 v[162:163], v25 offset:520\n"
    "ds_read_b128 v[76:79], v22 offset:576\n"
    "s_waitcnt lgkmcnt(15)\n"
    "v_mfma_f32_16x16x32_bf16 v[124:127], v[128:131], v[104:107], v[124:127]\n"
    "ds_read_b64 v[164:165], v25 offset:0\n"
    "ds_read_b64 v[166:167], v25 offset:8\n"
    "v_mfma_f32_16x16x32_bf16 v[132:135], v[136:139], v[104:107], v[132:135]\n"
    "ds_read_b128 v[168:171], v22 offset:640\n"
    "ds_write_b16 v17, v28 offset:32\n"
    "ds_write_b16_d16_hi v26, v28 offset:32\n"
    "s_waitcnt lgkmcnt(15)\n"
    "v_mfma_f32_16x16x32_bf16 v[124:127], v[140:143], v[32:35], v[124:127]\n"
    "ds_write_b16 v17, v28 offset:222\n"
    "ds_write_b16_d16_hi v26, v28 offset:222\n"
    "ds_write_b16 v17, v30 offset:0\n"
    "v_mfma_f32_16x16x32_bf16 v[132:135], v[144:147], v[32:35], v[132:135]\n"
    "ds_read_b128 v[88:91], v22 offset:704\n"
    "ds_write_b16_d16_hi v26, v30 offset:0\n"
    "ds_write_b16 v17, v30 offset:190\n"
    "s_waitcnt lgkmcnt(15)\n"
    "v_mfma_f32_16x16x32_bf16 v[124:127], v[148:151], v[64:67], v[124:127]\n"
    "ds_write_b16_d16_hi v26, v30 offset:190\n"
    "v_mul_u32_u24 v59, v23, v59\n"
    "v_add_u32 v20, 1280, v27\n"
    "v_mfma_f32_16x16x32_bf16 v[132:135], v[152:155], v[64:67], v[132:135]\n"
    "ds_read_b128 v[104:107], v22 offset:768\n"
    "v_mfma_f32_16x16x32_bf16 v[172:175], v[60:63], v[156:159], 0\n"
    "s_waitcnt lgkmcnt(15)\n"
    "v_mfma_f32_16x16x32_bf16 v[124:127], v[0:3], v[156:159], v[124:127]\n"
    "s_waitcnt lgkmcnt(14)\n"
    "v_mfma_f32_16x16x32_bf16 v[132:135], v[160:163], v[156:159], v[132:135]\n"
    "s_waitcnt lgkmcnt(13)\n"
    "v_mfma_f32_16x16x32_bf16 v[132:135], v[80:83], v[76:79], v[132:135]\n"
    "s_waitcnt lgkmcnt(11)\n"
    "v_mfma_f32_16x16x32_bf16 v[32:35], v[164:167], v[156:159], 0\n"
    "ds_read_b128 v[16:19], v22 offset:832\n"
    "ds_read2_b32 v[176:177], v27 offset0:16 offset1:17\n"
    "v_mfma_f32_16x16x32_bf16 v[32:35], v[68:71], v[76:79], v[32:35]\n"
    "ds_read_b128 v[180:183], v22 offset:896\n"
    "v_cvt_pk_bf16_f32 v172, 0, v172\n"
    "s_waitcnt lgkmcnt(13)\n"
    "v_mfma_f32_16x16x32_bf16 v[32:35], v[112:115], v[168:171], v[32:35]\n"
    "ds_read_b128 v[64:67], v22 offset:960\n"
    "v_cvt_pk_bf16_f32 v184, 0, v124\n"
    "v_mfma_f32_16x16x32_bf16 v[132:135], v[96:99], v[168:171], v[132:135]\n"
    "ds_read2_b32 v[178:179], v27 offset0:96 offset1:97\n"
    "v_cvt_pk_bf16_f32 v185, 0, v125\n"
    "s_waitcnt lgkmcnt(9)\n"
    "v_mfma_f32_16x16x32_bf16 v[32:35], v[8:11], v[88:91], v[32:35]\n"
    "ds_read_b128 v[156:159], v22 offset:1024\n"
    "v_cvt_pk_bf16_f32 v186, 0, v126\n"
    "v_mfma_f32_16x16x32_bf16 v[132:135], v[116:119], v[88:91], v[132:135]\n"
    "ds_read2_b32 v[76:77], v31 offset0:16 offset1:17\n"
    "v_cvt_pk_bf16_f32 v187, 0, v127\n"
    "s_waitcnt lgkmcnt(7)\n"
    "v_mfma_f32_16x16x32_bf16 v[132:135], v[12:15], v[104:107], v[132:135]\n"
    "ds_read2_b32 v[78:79], v31 offset0:96 offset1:97\n"
    "v_cvt_pk_bf16_f32 v173, 0, v173\n"
    "v_mfma_f32_16x16x32_bf16 v[32:35], v[120:123], v[104:107], v[32:35]\n"
    "ds_read_b128 v[168:171], v22 offset:1088\n"
    "v_cvt_pk_bf16_f32 v174, 0, v174\n"
    "v_cvt_pk_bf16_f32 v175, 0, v175\n"
    "v_add_f32 v184, v184, v172\n"
    "v_add_f32 v185, v185, v173\n"
    "v_add_f32 v186, v186, v174\n"
    "v_add_f32 v187, v187, v175\n"
    "v_cvt_pk_bf16_f32 v88, v184, v185\n"
    "v_cvt_pk_bf16_f32 v89, v186, v187\n"
    "s_waitcnt lgkmcnt(8)\n"
    "v_mfma_f32_16x16x32_bf16 v[132:135], v[128:131], v[16:19], v[132:135]\n"
    "ds_write_b64 v24, v[88:89] offset:0\n"
    "v_mfma_f32_16x16x32_bf16 v[32:35], v[136:139], v[16:19], v[32:35]\n"
    "ds_read_b128 v[104:107], v22 offset:1152\n"
    "s_waitcnt lgkmcnt(8)\n"
    "v_mfma_f32_16x16x32_bf16 v[132:135], v[140:143], v[180:183], v[132:135]\n"
    "v_mfma_f32_16x16x32_bf16 v[32:35], v[144:147], v[180:183], v[32:35]\n"
    "ds_read_b128 v[124:127], v22 offset:1216\n"
    "s_waitcnt lgkmcnt(8)\n"
    "v_mfma_f32_16x16x32_bf16 v[132:135], v[148:151], v[64:67], v[132:135]\n"
    "v_mfma_f32_16x16x32_bf16 v[32:35], v[152:155], v[64:67], v[32:35]\n"
    "ds_read_b128 v[172:175], v22 offset:1280\n"
    "s_waitcnt lgkmcnt(7)\n"
    "v_mfma_f32_16x16x32_bf16 v[188:191], v[60:63], v[156:159], 0\n"
    "v_mfma_f32_16x16x32_bf16 v[132:135], v[0:3], v[156:159], v[132:135]\n"
    "v_mfma_f32_16x16x32_bf16 v[32:35], v[160:163], v[156:159], v[32:35]\n"
    "v_mfma_f32_16x16x32_bf16 v[88:91], v[164:167], v[156:159], 0\n"
    "ds_read_b128 v[16:19], v22 offset:1344\n"
    "ds_read2_b32 v[184:185], v27 offset0:8 offset1:9\n"
    "s_waitcnt lgkmcnt(6)\n"
    "v_mfma_f32_16x16x32_bf16 v[32:35], v[80:83], v[168:171], v[32:35]\n"
    "ds_read2_b32 v[186:187], v27 offset0:88 offset1:89\n"
    "ds_read2_b32 v[180:181], v31 offset0:8 offset1:9\n"
    "v_mfma_f32_16x16x32_bf16 v[88:91], v[68:71], v[168:171], v[88:91]\n"
    "ds_read_b128 v[64:67], v22 offset:1408\n"
    "v_cvt_pk_bf16_f32 v192, 0, v132\n"
    "s_waitcnt lgkmcnt(7)\n"
    "v_mfma_f32_16x16x32_bf16 v[32:35], v[96:99], v[104:107], v[32:35]\n"
    "v_cvt_pk_bf16_f32 v193, 0, v133\n"
    "v_cvt_pk_bf16_f32 v194, 0, v134\n"
    "v_mfma_f32_16x16x32_bf16 v[88:91], v[112:115], v[104:107], v[88:91]\n"
    "ds_read_b128 v[156:159], v22 offset:1472\n"
    "v_cvt_pk_bf16_f32 v195, 0, v135\n"
    "s_waitcnt lgkmcnt(7)\n"
    "v_mfma_f32_16x16x32_bf16 v[32:35], v[116:119], v[124:127], v[32:35]\n"
    "v_cvt_pk_bf16_f32 v188, 0, v188\n"
    "v_cvt_pk_bf16_f32 v189, 0, v189\n"
    "v_mfma_f32_16x16x32_bf16 v[88:91], v[8:11], v[124:127], v[88:91]\n"
    "ds_read_b128 v[168:171], v22 offset:1536\n"
    "v_cvt_pk_bf16_f32 v190, 0, v190\n"
    "s_waitcnt lgkmcnt(7)\n"
    "v_mfma_f32_16x16x32_bf16 v[32:35], v[12:15], v[172:175], v[32:35]\n"
    "v_cvt_pk_bf16_f32 v191, 0, v191\n"
    "v_add_f32 v192, v192, v188\n"
    "v_mfma_f32_16x16x32_bf16 v[88:91], v[120:123], v[172:175], v[88:91]\n"
    "ds_read_b128 v[104:107], v22 offset:1600\n"
    "v_add_f32 v193, v193, v189\n"
    "v_mfma_f32_16x16x32_bf16 v[32:35], v[100:103], v[176:179], v[32:35]\n"
    "v_add_f32 v194, v194, v190\n"
    "v_add_f32 v195, v195, v191\n"
    "v_mfma_f32_16x16x32_bf16 v[32:35], v[72:75], v[76:79], v[32:35]\n"
    "v_cvt_pk_bf16_f32 v124, v192, v193\n"
    "v_cvt_pk_bf16_f32 v125, v194, v195\n"
    "s_waitcnt lgkmcnt(7)\n"
    "v_mfma_f32_16x16x32_bf16 v[32:35], v[128:131], v[16:19], v[32:35]\n"
    "ds_write_b64 v24, v[124:125] offset:512\n"
    "ds_read2_b32 v[182:183], v31 offset0:88 offset1:89\n"
    "v_mfma_f32_16x16x32_bf16 v[88:91], v[136:139], v[16:19], v[88:91]\n"
    "ds_read_b128 v[132:135], v22 offset:1664\n"
    "ds_read_b128 v[172:175], v22 offset:16\n"
    "s_waitcnt lgkmcnt(7)\n"
    "v_mfma_f32_16x16x32_bf16 v[32:35], v[140:143], v[64:67], v[32:35]\n"
    "v_mfma_f32_16x16x32_bf16 v[88:91], v[144:147], v[64:67], v[88:91]\n"
    "ds_read_b128 v[188:191], v22 offset:1536\n"
    "ds_read_b128 v[196:199], v22 offset:1728\n"
    "s_waitcnt lgkmcnt(8)\n"
    "v_mfma_f32_16x16x32_bf16 v[32:35], v[148:151], v[156:159], v[32:35]\n"
    "s_waitcnt lgkmcnt(5)\n"
    "s_barrier\n"
    "s_setprio 1\n"
    "ds_read_b64_tr_b16 v[124:125], v21 offset:0\n"
    "v_mfma_f32_16x16x32_bf16 v[88:91], v[152:155], v[156:159], v[88:91]\n"
    "ds_read_b64_tr_b16 v[126:127], v21 offset:35072\n"
    "ds_read_b128 v[16:19], v22 offset:1792\n"
    "v_mfma_f32_16x16x32_bf16 v[192:195], v[60:63], v[168:171], 0\n"
    "ds_read_b128 v[200:203], v22 offset:1520\n"
    "v_mfma_f32_16x16x32_bf16 v[32:35], v[0:3], v[168:171], v[32:35]\n"
    "v_mfma_f32_16x16x32_bf16 v[88:91], v[160:163], v[168:171], v[88:91]\n"
    "v_mfma_f32_16x16x32_bf16 v[64:67], v[164:167], v[168:171], 0\n"
    "ds_read_b128 v[204:207], v22 offset:1856\n"
    "ds_read2_b32 v[208:209], v27 offset0:0 offset1:1\n"
    "v_mfma_f32_16x16x32_bf16 v[88:91], v[80:83], v[104:107], v[88:91]\n"
    "ds_read2_b32 v[210:211], v27 offset0:80 offset1:81\n"
    "v_cvt_pk_bf16_f32 v192, 0, v192\n"
    "v_mfma_f32_16x16x32_bf16 v[64:67], v[68:71], v[104:107], v[64:67]\n"
    "ds_read_b128 v[156:159], v22 offset:1920\n"
    "v_cvt_pk_bf16_f32 v212, 0, v32\n"
    "s_waitcnt lgkmcnt(11)\n"
    "v_mfma_f32_16x16x32_bf16 v[88:91], v[96:99], v[132:135], v[88:91]\n"
    "v_cvt_pk_bf16_f32 v213, 0, v33\n"
    "v_cvt_pk_bf16_f32 v214, 0, v34\n"
    "v_mfma_f32_16x16x32_bf16 v[64:67], v[112:115], v[132:135], v[64:67]\n"
    "s_waitcnt lgkmcnt(6)\n"
    "global_store_dwordx4 v29, v[124:127], s[34:35]\n"
    "s_add_u32 s34, s34, 1310720\n"
    "s_addc_u32 s35, s35, 0\n"
    "v_cvt_pk_bf16_f32 v215, 0, v35\n"
    "v_mfma_f32_16x16x32_bf16 v[168:171], v[188:191], v[36:39], 0\n"
    "ds_read_b128 v[216:219], v22 offset:1984\n"
    "v_cvt_pk_bf16_f32 v193, 0, v193\n"
    "s_setprio 0\n"
    "v_mfma_f32_16x16x32_bf16 v[88:91], v[116:119], v[196:199], v[88:91]\n"
    "v_cvt_pk_bf16_f32 v194, 0, v194\n"
    "v_cvt_pk_bf16_f32 v195, 0, v195\n"
    "v_mfma_f32_16x16x32_bf16 v[64:67], v[8:11], v[196:199], v[64:67]\n"
    "ds_read_b128 v[104:107], v22 offset:2048\n"
    "v_add_f32 v212, v212, v192\n"
    "v_mfma_f32_16x16x32_bf16 v[124:127], v[188:191], v[40:43], 0\n"
    "v_add_f32 v213, v213, v193\n"
    "v_add_f32 v214, v214, v194\n"
    "s_waitcnt lgkmcnt(7)\n"
    "v_mfma_f32_16x16x32_bf16 v[88:91], v[12:15], v[16:19], v[88:91]\n"
    "v_add_f32 v215, v215, v195\n"
    "v_cvt_pk_bf16_f32 v26, v212, v213\n"
    "v_mfma_f32_16x16x32_bf16 v[64:67], v[120:123], v[16:19], v[64:67]\n"
    "v_cvt_pk_bf16_f32 v27, v214, v215\n"
    "v_cvt_pk_bf16_f32 v32, v168, v169\n"
    "v_mfma_f32_16x16x32_bf16 v[132:135], v[188:191], v[44:47], 0\n"
    "ds_write_b64 v24, v[26:27] offset:1024\n"
    "v_cvt_pk_bf16_f32 v33, v170, v171\n"
    "v_mfma_f32_16x16x32_bf16 v[88:91], v[4:7], v[176:179], v[88:91]\n"
    "v_cvt_pk_bf16_f32 v34, v124, v125\n"
    "v_cvt_pk_bf16_f32 v35, v126, v127\n"
    "v_mfma_f32_16x16x32_bf16 v[88:91], v[84:87], v[76:79], v[88:91]\n"
    "ds_read2_b32 v[192:193], v31 offset0:0 offset1:1\n"
    "ds_read2_b32 v[194:195], v31 offset0:80 offset1:81\n"
    "v_mfma_f32_16x16x32_bf16 v[88:91], v[100:103], v[184:187], v[88:91]\n"
    "v_cvt_pk_bf16_f32 v16, v132, v133\n"
    "s_waitcnt lgkmcnt(2)\n"
    "s_barrier\n"
    "s_setprio 1\n"
    "v_mfma_f32_16x16x32_bf16 v[88:91], v[72:75], v[180:183], v[88:91]\n"
    "ds_read_b64_tr_b16 v[168:169], v21 offset:1024\n"
    "ds_read_b64_tr_b16 v[170:171], v21 offset:36096\n"
    "v_mfma_f32_16x16x32_bf16 v[88:91], v[56:59], v[172:175], v[88:91]\n"
    "ds_read_b128 v[124:127], v22 offset:2112\n"
    "v_cvt_pk_bf16_f32 v17, v134, v135\n"
    "v_mfma_f32_16x16x32_bf16 v[88:91], v[128:131], v[204:207], v[88:91]\n"
    "ds_read2_b32 v[196:197], v20 offset0:24 offset1:25\n"
    "ds_read2_b32 v[198:199], v20 offset0:112 offset1:113\n"
    "v_mfma_f32_16x16x32_bf16 v[64:67], v[136:139], v[204:207], v[64:67]\n"
    "ds_read_b128 v[212:215], v22 offset:2176\n"
    "ds_read_b128 v[220:223], v22 offset:528\n"
    "v_mfma_f32_16x16x32_bf16 v[224:227], v[200:203], v[40:43], 0\n"
    "v_mfma_f32_16x16x32_bf16 v[88:91], v[140:143], v[156:159], v[88:91]\n"
    "v_mfma_f32_16x16x32_bf16 v[64:67], v[144:147], v[156:159], v[64:67]\n"
    "ds_read_b128 v[172:175], v22 offset:2048\n"
    "ds_read_b128 v[228:231], v22 offset:2240\n"
    "v_mfma_f32_16x16x32_bf16 v[132:135], v[200:203], v[48:51], 0\n"
    "ds_read_b128 v[232:235], v22 offset:2032\n"
    "v_mfma_f32_16x16x32_bf16 v[88:91], v[148:151], v[216:219], v[88:91]\n"
    "v_mfma_f32_16x16x32_bf16 v[64:67], v[152:155], v[216:219], v[64:67]\n"
    "s_waitcnt lgkmcnt(8)\n"
    "global_store_dwordx4 v29, v[168:171], s[34:35]\n"
    "s_add_u32 s34, s34, 1310720\n"
    "s_addc_u32 s35, s35, 0\n"
    "v_cvt_pk_bf16_f32 v18, v224, v225\n"
    "v_mfma_f32_16x16x32_bf16 v[204:207], v[188:191], v[52:55], 0\n"
    "ds_read_b128 v[236:239], v22 offset:2304\n"
    "v_cvt_pk_bf16_f32 v19, v226, v227\n"
    "v_mfma_f32_16x16x32_bf16 v[156:159], v[60:63], v[104:107], 0\n"
    "v_cvt_pk_bf16_f32 v240, v132, v133\n"
    "v_cvt_pk_bf16_f32 v241, v134, v135\n"
    "v_mfma_f32_16x16x32_bf16 v[88:91], v[0:3], v[104:107], v[88:91]\n"
    "v_mfma_f32_16x16x32_bf16 v[64:67], v[160:163], v[104:107], v[64:67]\n"
    "v_mfma_f32_16x16x32_bf16 v[200:203], v[164:167], v[104:107], 0\n"
    "ds_read_b128 v[216:219], v22 offset:2368\n"
    "v_cvt_pk_bf16_f32 v242, v204, v205\n"
    "s_waitcnt lgkmcnt(9)\n"
    "v_mfma_f32_16x16x32_bf16 v[64:67], v[80:83], v[124:127], v[64:67]\n"
    "v_cvt_pk_bf16_f32 v243, v206, v207\n"
    "v_cvt_pk_bf16_f32 v156, 0, v156\n"
    "v_mfma_f32_16x16x32_bf16 v[200:203], v[68:71], v[124:127], v[200:203]\n"
    "ds_read_b128 v[168:171], v22 offset:2432\n"
    "v_cvt_pk_bf16_f32 v188, 0, v88\n"
    "s_waitcnt lgkmcnt(7)\n"
    "v_mfma_f32_16x16x32_bf16 v[64:67], v[96:99], v[212:215], v[64:67]\n"
    "v_cvt_pk_bf16_f32 v189, 0, v89\n"
    "v_cvt_pk_bf16_f32 v190, 0, v90\n"
    "v_mfma_f32_16x16x32_bf16 v[200:203], v[112:115], v[212:215], v[200:203]\n"
    "ds_read_b128 v[104:107], v22 offset:2496\n"
    "v_cvt_pk_bf16_f32 v191, 0, v91\n"
    "s_waitcnt lgkmcnt(6)\n"
    "v_mfma_f32_16x16x32_bf16 v[132:135], v[172:175], v[36:39], 0\n"
    "v_cvt_pk_bf16_f32 v157, 0, v157\n"
    "v_cvt_pk_bf16_f32 v158, 0, v158\n"
    "s_waitcnt lgkmcnt(5)\n"
    "v_mfma_f32_16x16x32_bf16 v[64:67], v[116:119], v[228:231], v[64:67]\n"
    "v_cvt_pk_bf16_f32 v159, 0, v159\n"
    "v_add_f32 v188, v188, v156\n"
    "v_mfma_f32_16x16x32_bf16 v[200:203], v[8:11], v[228:231], v[200:203]\n"
    "ds_read_b128 v[124:127], v22 offset:2560\n"
    "v_add_f32 v189, v189, v157\n"
    "v_mfma_f32_16x16x32_bf16 v[204:207], v[172:175], v[40:43], 0\n"
    "v_add_f32 v190, v190, v158\n"
    "v_add_f32 v191, v191, v159\n"
    "s_waitcnt lgkmcnt(4)\n"
    "v_mfma_f32_16x16x32_bf16 v[64:67], v[12:15], v[236:239], v[64:67]\n"
    "v_cvt_pk_bf16_f32 v26, v188, v189\n"
    "v_cvt_pk_bf16_f32 v27, v190, v191\n"
    "v_mfma_f32_16x16x32_bf16 v[200:203], v[120:123], v[236:239], v[200:203]\n"
    "ds_write_b64 v24, v[26:27] offset:1536\n"
    "v_cvt_pk_bf16_f32 v88, v132, v133\n"
    "v_mfma_f32_16x16x32_bf16 v[212:215], v[172:175], v[44:47], 0\n"
    "ds_read_b128 v[224:227], v22 offset:2624\n"
    "v_cvt_pk_bf16_f32 v89, v134, v135\n"
    "v_mfma_f32_16x16x32_bf16 v[64:67], v[32:35], v[176:179], v[64:67]\n"
    "v_cvt_pk_bf16_f32 v90, v204, v205\n"
    "v_cvt_pk_bf16_f32 v91, v206, v207\n"
    "v_mfma_f32_16x16x32_bf16 v[64:67], v[16:19], v[76:79], v[64:67]\n"
    "ds_read2_b32 v[156:157], v20 offset0:16 offset1:17\n"
    "ds_read2_b32 v[158:159], v20 offset0:104 offset1:105\n"
    "v_mfma_f32_16x16x32_bf16 v[64:67], v[4:7], v[184:187], v[64:67]\n"
    "s_setprio 0\n"
    "v_cvt_pk_bf16_f32 v188, v212, v213\n"
    "v_cvt_pk_bf16_f32 v189, v214, v215\n"
    "v_mfma_f32_16x16x32_bf16 v[64:67], v[84:87], v[180:183], v[64:67]\n"
    "ds_read_b128 v[132:135], v22 offset:1040\n"
    "v_mfma_f32_16x16x32_bf16 v[64:67], v[100:103], v[208:211], v[64:67]\n"
    "v_mfma_f32_16x16x32_bf16 v[64:67], v[72:75], v[192:195], v[64:67]\n"
    "v_mfma_f32_16x16x32_bf16 v[64:67], v[92:95], v[196:199], v[64:67]\n"
    "v_mfma_f32_16x16x32_bf16 v[64:67], v[56:59], v[220:223], v[64:67]\n"
    "s_waitcnt lgkmcnt(8)\n"
    "v_mfma_f32_16x16x32_bf16 v[64:67], v[128:131], v[216:219], v[64:67]\n"
    "v_mfma_f32_16x16x32_bf16 v[200:203], v[136:139], v[216:219], v[200:203]\n"
    "ds_read_b128 v[204:207], v22 offset:2688\n"
    "v_mfma_f32_16x16x32_bf16 v[228:231], v[232:235], v[40:43], 0\n"
    "s_waitcnt lgkmcnt(8)\n"
    "v_mfma_f32_16x16x32_bf16 v[64:67], v[140:143], v[168:171], v[64:67]\n"
    "v_mfma_f32_16x16x32_bf16 v[200:203], v[144:147], v[168:171], v[200:203]\n"
    "ds_read_b128 v[212:215], v22 offset:2560\n"
    "ds_read_b128 v[236:239], v22 offset:2752\n"
    "v_mfma_f32_16x16x32_bf16 v[100:103], v[232:235], v[48:51], 0\n"
    "ds_read_b128 v[72:75], v22 offset:2544\n"
    "s_waitcnt lgkmcnt(10)\n"
    "v_mfma_f32_16x16x32_bf16 v[64:67], v[148:151], v[104:107], v[64:67]\n"
    "v_mfma_f32_16x16x32_bf16 v[200:203], v[152:155], v[104:107], v[200:203]\n"
    "ds_read_b128 v[220:223], v22 offset:2816\n"
    "v_cvt_pk_bf16_f32 v190, v228, v229\n"
    "v_mfma_f32_16x16x32_bf16 v[216:219], v[172:175], v[52:55], 0\n"
    "v_cvt_pk_bf16_f32 v191, v230, v231\n"
    "s_waitcnt lgkmcnt(10)\n"
    "v_mfma_f32_16x16x32_bf16 v[244:247], v[60:63], v[124:127], 0\n"
    "v_cvt_pk_bf16_f32 v168, v100, v101\n"
    "v_cvt_pk_bf16_f32 v169, v102, v103\n"
    "v_mfma_f32_16x16x32_bf16 v[64:67], v[0:3], v[124:127], v[64:67]\n"
    "v_mfma_f32_16x16x32_bf16 v[200:203], v[160:163], v[124:127], v[200:203]\n"
    "v_mfma_f32_16x16x32_bf16 v[48:51], v[164:167], v[124:127], 0\n"
    "ds_read_b128 v[232:235], v22 offset:2880\n"
    "v_cvt_pk_bf16_f32 v170, v216, v217\n"
    "s_waitcnt lgkmcnt(9)\n"
    "v_mfma_f32_16x16x32_bf16 v[200:203], v[80:83], v[224:227], v[200:203]\n"
    "v_cvt_pk_bf16_f32 v171, v218, v219\n"
    "v_cvt_pk_bf16_f32 v244, 0, v244\n"
    "v_mfma_f32_16x16x32_bf16 v[48:51], v[68:71], v[224:227], v[48:51]\n"
    "ds_read_b128 v[52:55], v22 offset:2944\n"
    "v_cvt_pk_bf16_f32 v104, 0, v64\n"
    "s_waitcnt lgkmcnt(6)\n"
    "v_mfma_f32_16x16x32_bf16 v[200:203], v[96:99], v[204:207], v[200:203]\n"
    "v_cvt_pk_bf16_f32 v105, 0, v65\n"
    "v_cvt_pk_bf16_f32 v106, 0, v66\n"
    "v_mfma_f32_16x16x32_bf16 v[48:51], v[112:115], v[204:207], v[48:51]\n"
    "ds_read_b128 v[100:103], v22 offset:3008\n"
    "v_cvt_pk_bf16_f32 v107, 0, v67\n"
    "s_waitcnt lgkmcnt(6)\n"
    "v_mfma_f32_16x16x32_bf16 v[124:127], v[212:215], v[36:39], 0\n"
    "v_cvt_pk_bf16_f32 v245, 0, v245\n"
    "v_cvt_pk_bf16_f32 v246, 0, v246\n"
    "s_waitcnt lgkmcnt(5)\n"
    "v_mfma_f32_16x16x32_bf16 v[200:203], v[116:119], v[236:239], v[200:203]\n"
    "v_cvt_pk_bf16_f32 v247, 0, v247\n"
    "v_add_f32 v104, v104, v244\n"
    "v_mfma_f32_16x16x32_bf16 v[48:51], v[8:11], v[236:239], v[48:51]\n"
    "ds_read_b128 v[172:175], v22 offset:3072\n"
    "v_add_f32 v105, v105, v245\n"
    "v_mfma_f32_16x16x32_bf16 v[204:207], v[212:215], v[40:43], 0\n"
    "v_add_f32 v106, v106, v246\n"
    "v_add_f32 v107, v107, v247\n"
    "s_waitcnt lgkmcnt(4)\n"
    "v_mfma_f32_16x16x32_bf16 v[200:203], v[12:15], v[220:223], v[200:203]\n"
    "v_cvt_pk_bf16_f32 v26, v104, v105\n"
    "v_cvt_pk_bf16_f32 v27, v106, v107\n"
    "v_mfma_f32_16x16x32_bf16 v[48:51], v[120:123], v[220:223], v[48:51]\n"
    "ds_write_b64 v24, v[26:27] offset:2048\n"
    "v_cvt_pk_bf16_f32 v64, v124, v125\n"
    "v_mfma_f32_16x16x32_bf16 v[216:219], v[212:215], v[44:47], 0\n"
    "ds_read_b128 v[224:227], v22 offset:3136\n"
    "v_cvt_pk_bf16_f32 v65, v126, v127\n"
    "v_mfma_f32_16x16x32_bf16 v[200:203], v[88:91], v[176:179], v[200:203]\n"
    "v_cvt_pk_bf16_f32 v66, v204, v205\n"
    "v_cvt_pk_bf16_f32 v67, v206, v207\n"
    "v_mfma_f32_16x16x32_bf16 v[200:203], v[188:191], v[76:79], v[200:203]\n"
    "ds_read2_b32 v[228:229], v20 offset0:8 offset1:9\n"
    "ds_read2_b32 v[230:231], v20 offset0:96 offset1:97\n"
    "v_mfma_f32_16x16x32_bf16 v[200:203], v[32:35], v[184:187], v[200:203]\n"
    "ds_read_b128 v[104:107], v22 offset:1552\n"
    "v_cvt_pk_bf16_f32 v220, v216, v217\n"
    "v_mfma_f32_16x16x32_bf16 v[200:203], v[16:19], v[180:183], v[200:203]\n"
    "s_waitcnt lgkmcnt(4)\n"
    "s_barrier\n"
    "s_setprio 1\n"
    "ds_read_b64_tr_b16 v[124:125], v21 offset:2048\n"
    "v_mfma_f32_16x16x32_bf16 v[200:203], v[4:7], v[208:211], v[200:203]\n"
    "ds_read_b64_tr_b16 v[126:127], v21 offset:37120\n"
    "v_cvt_pk_bf16_f32 v221, v218, v219\n"
    "v_mfma_f32_16x16x32_bf16 v[200:203], v[84:87], v[192:195], v[200:203]\n"
    "v_mfma_f32_16x16x32_bf16 v[200:203], v[108:111], v[196:199], v[200:203]\n"
    "v_mfma_f32_16x16x32_bf16 v[200:203], v[92:95], v[156:159], v[200:203]\n"
    "v_mfma_f32_16x16x32_bf16 v[200:203], v[56:59], v[132:135], v[200:203]\n"
    "v_mfma_f32_16x16x32_bf16 v[200:203], v[128:131], v[232:235], v[200:203]\n"
    "v_mfma_f32_16x16x32_bf16 v[48:51], v[136:139], v[232:235], v[48:51]\n"
    "ds_read_b128 v[204:207], v22 offset:3200\n"
    "v_mfma_f32_16x16x32_bf16 v[212:215], v[72:75], v[40:43], 0\n"
    "v_mfma_f32_16x16x32_bf16 v[48:51], v[144:147], v[52:55], v[48:51]\n"
    "s_waitcnt lgkmcnt(1)\n"
    "global_store_dwordx4 v29, v[124:127], s[34:35]\n"
    "s_add_u32 s34, s34, 1310720\n"
    "s_addc_u32 s35, s35, 0\n"
    "ds_read_b128 v[4:7], v22 offset:3264\n"
    "v_mfma_f32_16x16x32_bf16 v[200:203], v[140:143], v[52:55], v[200:203]\n"
    "v_mfma_f32_16x16x32_bf16 v[48:51], v[152:155], v[100:103], v[48:51]\n"
    "ds_read_b128 v[84:87], v22 offset:3328\n"
    "v_mfma_f32_16x16x32_bf16 v[200:203], v[148:151], v[100:103], v[200:203]\n"
    "v_mfma_f32_16x16x32_bf16 v[132:135], v[60:63], v[172:175], 0\n"
    "v_cvt_pk_bf16_f32 v222, v212, v213\n"
    "v_cvt_pk_bf16_f32 v223, v214, v215\n"
    "v_mfma_f32_16x16x32_bf16 v[200:203], v[0:3], v[172:175], v[200:203]\n"
    "v_mfma_f32_16x16x32_bf16 v[48:51], v[160:163], v[172:175], v[48:51]\n"
    "v_mfma_f32_16x16x32_bf16 v[72:75], v[164:167], v[172:175], 0\n"
    "ds_read_b128 v[124:127], v22 offset:3392\n"
    "ds_read2_b32 v[216:217], v20 offset0:0 offset1:1\n"
    "v_mfma_f32_16x16x32_bf16 v[48:51], v[80:83], v[224:227], v[48:51]\n"
    "ds_read_b128 v[52:55], v22 offset:3456\n"
    "v_cvt_pk_bf16_f32 v132, 0, v132\n"
    "s_waitcnt lgkmcnt(5)\n"
    "v_mfma_f32_16x16x32_bf16 v[48:51], v[96:99], v[204:207], v[48:51]\n"
    "s_setprio 0\n"
    "ds_read_b128 v[100:103], v22 offset:3520\n"
    "v_cvt_pk_bf16_f32 v212, 0, v200\n"
    "v_cvt_pk_bf16_f32 v213, 0, v201\n"
    "v_cvt_pk_bf16_f32 v214, 0, v202\n"
    "s_waitcnt lgkmcnt(5)\n"
    "v_mfma_f32_16x16x32_bf16 v[48:51], v[116:119], v[4:7], v[48:51]\n"
    "ds_read_b128 v[164:167], v22 offset:3584\n"
    "v_cvt_pk_bf16_f32 v215, 0, v203\n"
    "v_cvt_pk_bf16_f32 v133, 0, v133\n"
    "v_cvt_pk_bf16_f32 v134, 0, v134\n"
    "s_waitcnt lgkmcnt(5)\n"
    "v_mfma_f32_16x16x32_bf16 v[48:51], v[12:15], v[84:87], v[48:51]\n"
    "ds_read_b128 v[172:175], v22 offset:3072\n"
    "v_cvt_pk_bf16_f32 v135, 0, v135\n"
    "v_mfma_f32_16x16x32_bf16 v[48:51], v[64:67], v[176:179], v[48:51]\n"
    "ds_read_b128 v[232:235], v22 offset:3056\n"
    "v_add_f32 v212, v212, v132\n"
    "v_mfma_f32_16x16x32_bf16 v[48:51], v[220:223], v[76:79], v[48:51]\n"
    "ds_read_b128 v[236:239], v22 offset:4096\n"
    "v_add_f32 v213, v213, v133\n"
    "v_mfma_f32_16x16x32_bf16 v[48:51], v[88:91], v[184:187], v[48:51]\n"
    "v_add_f32 v214, v214, v134\n"
    "v_add_f32 v215, v215, v135\n"
    "v_mfma_f32_16x16x32_bf16 v[48:51], v[188:191], v[180:183], v[48:51]\n"
    "v_cvt_pk_bf16_f32 v26, v212, v213\n"
    "v_cvt_pk_bf16_f32 v27, v214, v215\n"
    "v_mfma_f32_16x16x32_bf16 v[48:51], v[32:35], v[208:211], v[48:51]\n"
    "ds_write_b64 v24, v[26:27] offset:2560\n"
    "ds_read2_b32 v[218:219], v20 offset0:88 offset1:89\n"
    "v_mfma_f32_16x16x32_bf16 v[48:51], v[16:19], v[192:195], v[48:51]\n"
    "ds_read_b128 v[200:203], v22 offset:2064\n"
    "v_mfma_f32_16x16x32_bf16 v[48:51], v[240:243], v[196:199], v[48:51]\n"
    "v_mfma_f32_16x16x32_bf16 v[48:51], v[108:111], v[156:159], v[48:51]\n"
    "v_mfma_f32_16x16x32_bf16 v[48:51], v[92:95], v[228:231], v[48:51]\n"
    "s_waitcnt lgkmcnt(2)\n"
    "s_barrier\n"
    "s_setprio 1\n"
    "v_mfma_f32_16x16x32_bf16 v[48:51], v[56:59], v[104:107], v[48:51]\n"
    "v_mfma_f32_16x16x32_bf16 v[48:51], v[128:131], v[124:127], v[48:51]\n"
    "ds_read_b128 v[32:35], v22 offset:3648\n"
    "v_mfma_f32_16x16x32_bf16 v[48:51], v[140:143], v[52:55], v[48:51]\n"
    "ds_read_b128 v[132:135], v22 offset:3712\n"
    "v_mfma_f32_16x16x32_bf16 v[48:51], v[148:151], v[100:103], v[48:51]\n"
    "ds_read_b128 v[16:19], v22 offset:3776\n"
    "v_mfma_f32_16x16x32_bf16 v[212:215], v[60:63], v[164:167], 0\n"
    "v_mfma_f32_16x16x32_bf16 v[48:51], v[0:3], v[164:167], v[48:51]\n"
    "ds_read_b128 v[244:247], v22 offset:3840\n"
    "v_mfma_f32_16x16x32_bf16 v[72:75], v[68:71], v[224:227], v[72:75]\n"
    "v_mfma_f32_16x16x32_bf16 v[72:75], v[112:115], v[204:207], v[72:75]\n"
    "v_mfma_f32_16x16x32_bf16 v[72:75], v[8:11], v[4:7], v[72:75]\n"
    "v_mfma_f32_16x16x32_bf16 v[72:75], v[120:123], v[84:87], v[72:75]\n"
    "v_mfma_f32_16x16x32_bf16 v[72:75], v[136:139], v[124:127], v[72:75]\n"
    "v_mfma_f32_16x16x32_bf16 v[72:75], v[144:147], v[52:55], v[72:75]\n"
    "v_cvt_pk_bf16_f32 v212, 0, v212\n"
    "v_cvt_pk_bf16_f32 v104, 0, v48\n"
    "v_mfma_f32_16x16x32_bf16 v[72:75], v[152:155], v[100:103], v[72:75]\n"
    "v_cvt_pk_bf16_f32 v105, 0, v49\n"
    "v_cvt_pk_bf16_f32 v106, 0, v50\n"
    "v_mfma_f32_16x16x32_bf16 v[72:75], v[160:163], v[164:167], v[72:75]\n"
    "v_cvt_pk_bf16_f32 v107, 0, v51\n"
    "s_setprio 0\n"
    "v_cvt_pk_bf16_f32 v213, 0, v213\n"
    "v_mfma_f32_16x16x32_bf16 v[4:7], v[172:175], v[36:39], 0\n"
    "v_cvt_pk_bf16_f32 v214, 0, v214\n"
    "v_cvt_pk_bf16_f32 v215, 0, v215\n"
    "v_mfma_f32_16x16x32_bf16 v[8:11], v[172:175], v[40:43], 0\n"
    "v_add_f32 v104, v104, v212\n"
    "v_add_f32 v105, v105, v213\n"
    "v_mfma_f32_16x16x32_bf16 v[52:55], v[172:175], v[44:47], 0\n"
    "v_add_f32 v106, v106, v214\n"
    "v_add_f32 v107, v107, v215\n"
    "v_mfma_f32_16x16x32_bf16 v[48:51], v[232:235], v[40:43], 0\n"
    "v_cvt_pk_bf16_f32 v26, v104, v105\n"
    "v_cvt_pk_bf16_f32 v27, v106, v107\n"
    "v_mfma_f32_16x16x32_bf16 v[72:75], v[0:3], v[236:239], v[72:75]\n"
    "ds_write_b64 v24, v[26:27] offset:3072\n"
    "ds_read_b128 v[36:39], v22 offset:3904\n"
    "v_mfma_f32_16x16x32_bf16 v[68:71], v[60:63], v[236:239], 0\n"
    "v_cvt_pk_bf16_f32 v84, v4, v5\n"
    "v_cvt_pk_bf16_f32 v85, v6, v7\n"
    "s_waitcnt lgkmcnt(5)\n"
    "v_mfma_f32_16x16x32_bf16 v[72:75], v[80:83], v[32:35], v[72:75]\n"
    "ds_read_b128 v[44:47], v22 offset:3968\n"
    "v_cvt_pk_bf16_f32 v86, v8, v9\n"
    "s_waitcnt lgkmcnt(5)\n"
    "v_mfma_f32_16x16x32_bf16 v[72:75], v[96:99], v[132:135], v[72:75]\n"
    "ds_read_b128 v[40:43], v22 offset:4032\n"
    "s_waitcnt lgkmcnt(3)\n"
    "s_barrier\n"
    "s_setprio 1\n"
    "v_mfma_f32_16x16x32_bf16 v[72:75], v[116:119], v[16:19], v[72:75]\n"
    "ds_read_b64_tr_b16 v[0:1], v21 offset:3072\n"
    "ds_read_b64_tr_b16 v[2:3], v21 offset:38144\n"
    "v_cvt_pk_bf16_f32 v87, v10, v11\n"
    "v_cvt_pk_bf16_f32 v4, v52, v53\n"
    "v_cvt_pk_bf16_f32 v5, v54, v55\n"
    "v_mfma_f32_16x16x32_bf16 v[72:75], v[84:87], v[176:179], v[72:75]\n"
    "v_cvt_pk_bf16_f32 v6, v48, v49\n"
    "v_cvt_pk_bf16_f32 v7, v50, v51\n"
    "v_cvt_pk_bf16_f32 v68, 0, v68\n"
    "v_cvt_pk_bf16_f32 v69, 0, v69\n"
    "v_mfma_f32_16x16x32_bf16 v[72:75], v[4:7], v[76:79], v[72:75]\n"
    "v_cvt_pk_bf16_f32 v70, 0, v70\n"
    "v_cvt_pk_bf16_f32 v71, 0, v71\n"
    "v_mfma_f32_16x16x32_bf16 v[72:75], v[64:67], v[184:187], v[72:75]\n"
    "v_mfma_f32_16x16x32_bf16 v[72:75], v[220:223], v[180:183], v[72:75]\n"
    "v_mfma_f32_16x16x32_bf16 v[72:75], v[88:91], v[208:211], v[72:75]\n"
    "v_mfma_f32_16x16x32_bf16 v[72:75], v[188:191], v[192:195], v[72:75]\n"
    "v_mfma_f32_16x16x32_bf16 v[72:75], v[168:171], v[196:199], v[72:75]\n"
    "s_setprio 0\n"
    "s_waitcnt lgkmcnt(0)\n"
    "global_store_dwordx4 v29, v[0:3], s[34:35]\n"
    "s_add_u32 s34, s34, 1310720\n"
    "s_addc_u32 s35, s35, 0\n"
    "v_mfma_f32_16x16x32_bf16 v[72:75], v[240:243], v[156:159], v[72:75]\n"
    "v_mfma_f32_16x16x32_bf16 v[72:75], v[108:111], v[228:231], v[72:75]\n"
    "v_mfma_f32_16x16x32_bf16 v[72:75], v[92:95], v[216:219], v[72:75]\n"
    "v_mfma_f32_16x16x32_bf16 v[72:75], v[56:59], v[200:203], v[72:75]\n"
    "v_mfma_f32_16x16x32_bf16 v[72:75], v[12:15], v[244:247], v[72:75]\n"
    "v_mfma_f32_16x16x32_bf16 v[72:75], v[128:131], v[36:39], v[72:75]\n"
    "v_mfma_f32_16x16x32_bf16 v[72:75], v[140:143], v[44:47], v[72:75]\n"
    "v_mfma_f32_16x16x32_bf16 v[72:75], v[148:151], v[40:43], v[72:75]\n"
    "s_nop 7\n"
    "v_cvt_pk_bf16_f32 v4, 0, v72\n"
    "v_add_f32 v4, v4, v68\n"
    "v_cvt_pk_bf16_f32 v5, 0, v73\n"
    "v_add_f32 v5, v5, v69\n"
    "v_cvt_pk_bf16_f32 v26, v4, v5\n"
    "v_cvt_pk_bf16_f32 v6, 0, v74\n"
    "v_add_f32 v6, v6, v70\n"
    "v_cvt_pk_bf16_f32 v7, 0, v75\n"
    "v_add_f32 v7, v7, v71\n"
    "v_cvt_pk_bf16_f32 v27, v6, v7\n"
    "ds_write_b64 v24, v[26:27] offset:3584\n"
    "s_waitcnt lgkmcnt(0)\n"
    "s_barrier\n"
    "s_endpgm\n"
    ".Lhelp%=:\n"
    "s_sub_u32 s10, 31, s13\n"
    "s_min_u32 s10, s10, 11\n"
    "s_mul_i32 s10, s10, 86\n"
    "s_and_b32 s11, s8, 3\n"
    "s_mul_i32 s11, s11, 320\n"
    "s_add_u32 s12, s11, 319\n"
    "s_min_u32 s12, s12, 1279\n"
    "s_and_b32 s11, s11, 0xf80\n"
    "v_lshrrev_b32 v4, 2, v0\n"
    "v_and_b32 v2, 3, v0\n"
    "v_lshl_add_u32 v2, v2, 7, s11\n"
    "v_min_u32 v2, s12, v2\n"
    "v_and_b32 v2, -4, v2\n"
    "v_add_u32 v4, s10, v4\n"
    "v_add_u32 v3, 0, v4\n"
    "v_min_u32 v3, 1023, v3\n"
    "v_mul_u32_u24 v3, 1280, v3\n"
    "v_add_u32 v6, v3, v2\n"
    "v_add_u32 v10, 1310720, v6\n"
    "s_waitcnt lgkmcnt(0)\n"
    "s_sleep 24\n"
    "global_load_dword v6, v6, s[30:31]\n"
    "s_sleep 20\n"
    "global_load_dword v10, v10, s[30:31]\n"
    "s_endpgm\n"
    ".Lhi%=:\n"
    "s_setprio 3\n"
    "v_lshlrev_b32 v64, 16, v18\n"
    "v_mul_f32 v34, v4, v34\n"
    "v_mul_f32 v64, v5, v64\n"
    "v_exp_f32 v65, v34\n"
    "s_lshl_b32 s37, s9, 3\n"
    "s_add_u32 s37, s37, 128\n"
    "s_load_dwordx2 s[32:33], %1, s37\n"
    "s_load_dwordx2 s[34:35], %1, 144\n"
    "v_fract_f32 v66, v64\n"
    "v_and_b32 v19, 0xffff0000, v18\n"
    "v_cos_f32 v68, v66\n"
    "v_sin_f32 v69, v66\n"
    "v_mul_f32 v19, v5, v19\n"
    "v_mul_f32 v70, v65, v68\n"
    "v_mul_f32 v71, v65, v69\n"
    "v_mul_f32 v35, v4, v35\n"
    "v_pk_mul_f32 v[0:1], v[70:71], v[70:71] op_sel_hi:[0,1]\n"
    "v_fract_f32 v3, v19\n"
    "v_pk_fma_f32 v[72:73], v[70:71], v[70:71], v[0:1] op_sel:[1,1,0] op_sel_hi:[1,0,1] neg_lo:[0,1,0]\n"
    "v_exp_f32 v74, v35\n"
    "v_pk_mul_f32 v[76:77], v[72:73], v[72:73] op_sel_hi:[0,1]\n"
    "v_cos_f32 v78, v3\n"
    "v_pk_fma_f32 v[68:69], v[72:73], v[72:73], v[76:77] op_sel:[1,1,0] op_sel_hi:[1,0,1] neg_lo:[0,1,0]\n"
    "v_sin_f32 v79, v3\n"
    "v_pk_mul_f32 v[66:67], v[68:69], v[68:69] op_sel_hi:[0,1]\n"
    "v_mul_f32 v64, v74, v78\n"
    "v_pk_fma_f32 v[80:81], v[68:69], v[68:69], v[66:67] op_sel:[1,1,0] op_sel_hi:[1,0,1] neg_lo:[0,1,0]\n"
    "v_mul_f32 v65, v74, v79\n"
    "v_pk_mul_f32 v[82:83], v[80:81], v[80:81] op_sel_hi:[0,1]\n"
    "v_cvt_pk_f16_f32 v18, v80, -v81\n"
    "v_pk_fma_f32 v[0:1], v[80:81], v[80:81], v[82:83] op_sel:[1,1,0] op_sel_hi:[1,0,1] neg_lo:[0,1,0]\n"
    "ds_write_addtid_b32 v30 offset:0\n"
    "ds_write_addtid_b32 v18 offset:272\n"
    "v_cvt_pk_f16_f32 v19, v0, -v1\n"
    "v_pk_mul_f32 v[76:77], v[0:1], v[0:1] op_sel_hi:[0,1]\n"
    "v_pk_mul_f16 v3, v18, v19 op_sel_hi:[0,1]\n"
    "v_pk_fma_f32 v[84:85], v[0:1], v[0:1], v[76:77] op_sel:[1,1,0] op_sel_hi:[1,0,1] neg_lo:[0,1,0]\n"
    "v_pk_fma_f16 v75, v18, v19, v3 op_sel:[1,1,0] op_sel_hi:[1,0,1] neg_lo:[0,1,0]\n"
    "ds_write_addtid_b32 v19 offset:544\n"
    "ds_write_addtid_b32 v75 offset:816\n"
    "v_cvt_pk_f16_f32 v74, v84, -v85\n"
    "v_pk_mul_f32 v[66:67], v[84:85], v[84:85] op_sel_hi:[0,1]\n"
    "v_pk_mul_f16 v86, v18, v74 op_sel_hi:[0,1]\n"
    "v_pk_mul_f16 v30, v19, v74 op_sel_hi:[0,1]\n"
    "v_pk_fma_f16 v87, v18, v74, v86 op_sel:[1,1,0] op_sel_hi:[1,0,1] neg_lo:[0,1,0]\n"
    "ds_write_addtid_b32 v74 offset:1088\n"
    "ds_write_addtid_b32 v87 offset:1360\n"
    "v_pk_mul_f16 v78, v75, v74 op_sel_hi:[0,1]\n"
    "v_pk_fma_f16 v0, v19, v74, v30 op_sel:[1,1,0] op_sel_hi:[1,0,1] neg_lo:[0,1,0]\n"
    "v_pk_fma_f32 v[76:77], v[84:85], v[84:85], v[66:67] op_sel:[1,1,0] op_sel_hi:[1,0,1] neg_lo:[0,1,0]\n"
    "v_pk_fma_f16 v79, v75, v74, v78 op_sel:[1,1,0] op_sel_hi:[1,0,1] neg_lo:[0,1,0]\n"
    "ds_write_addtid_b32 v0 offset:1632\n"
    "ds_write_addtid_b32 v79 offset:1904\n"
    "v_cvt_pk_f16_f32 v1, v76, -v77\n"
    "v_pk_mul_f32 v[80:81], v[76:77], v[76:77] op_sel_hi:[0,1]\n"
    "v_pk_mul_f16 v3, v1, v18 op_sel_hi:[0,1]\n"
    "v_pk_mul_f16 v82, v1, v19 op_sel_hi:[0,1]\n"
    "v_pk_fma_f16 v83, v1, v18, v3 op_sel:[1,1,0] op_sel_hi:[1,0,1] neg_lo:[0,1,0]\n"
    "ds_write_addtid_b32 v1 offset:2176\n"
    "ds_write_addtid_b32 v83 offset:2448\n"
    "v_pk_mul_f16 v30, v1, v75 op_sel_hi:[0,1]\n"
    "v_pk_fma_f16 v66, v1, v19, v82 op_sel:[1,1,0] op_sel_hi:[1,0,1] neg_lo:[0,1,0]\n"
    "v_pk_fma_f16 v67, v1, v75, v30 op_sel:[1,1,0] op_sel_hi:[1,0,1] neg_lo:[0,1,0]\n"
    "v_pk_fma_f32 v[84:85], v[76:77], v[76:77], v[80:81] op_sel:[1,1,0] op_sel_hi:[1,0,1] neg_lo:[0,1,0]\n"
    "ds_write_addtid_b32 v66 offset:2720\n"
    "ds_write_addtid_b32 v67 offset:2992\n"
    "v_pk_mul_f16 v86, v1, v74 op_sel_hi:[0,1]\n"
    "v_cvt_pk_f16_f32 v78, v84, -v85\n"
    "v_pk_fma_f16 v88, v1, v74, v86 op_sel:[1,1,0] op_sel_hi:[1,0,1] neg_lo:[0,1,0]\n"
    "v_pk_mul_f16 v3, v78, v18 op_sel_hi:[0,1]\n"
    "ds_write_addtid_b32 v78 offset:4352\n"
    "ds_write_addtid_b32 v88 offset:3264\n"
    "v_pk_mul_f16 v83, v1, v87 op_sel_hi:[0,1]\n"
    "v_pk_fma_f16 v82, v78, v18, v3 op_sel:[1,1,0] op_sel_hi:[1,0,1] neg_lo:[0,1,0]\n"
    "v_pk_fma_f16 v30, v1, v87, v83 op_sel:[1,1,0] op_sel_hi:[1,0,1] neg_lo:[0,1,0]\n"
    "v_pk_mul_f16 v76, v78, v19 op_sel_hi:[0,1]\n"
    "ds_write_addtid_b32 v82 offset:4624\n"
    "ds_write_addtid_b32 v30 offset:3536\n"
    "v_pk_mul_f16 v77, v1, v0 op_sel_hi:[0,1]\n"
    "v_pk_fma_f16 v66, v78, v19, v76 op_sel:[1,1,0] op_sel_hi:[1,0,1] neg_lo:[0,1,0]\n"
    "v_pk_fma_f16 v67, v1, v0, v77 op_sel:[1,1,0] op_sel_hi:[1,0,1] neg_lo:[0,1,0]\n"
    "v_pk_mul_f16 v86, v78, v75 op_sel_hi:[0,1]\n"
    "ds_write_addtid_b32 v66 offset:4896\n"
    "ds_write_addtid_b32 v67 offset:3808\n"
    "v_pk_mul_f16 v80, v1, v79 op_sel_hi:[0,1]\n"
    "v_pk_fma_f16 v3, v78, v75, v86 op_sel:[1,1,0] op_sel_hi:[1,0,1] neg_lo:[0,1,0]\n"
    "v_pk_fma_f16 v81, v1, v79, v80 op_sel:[1,1,0] op_sel_hi:[1,0,1] neg_lo:[0,1,0]\n"
    "v_pk_mul_f16 v83, v1, v78 op_sel_hi:[0,1]\n"
    "ds_write_addtid_b32 v3 offset:5168\n"
    "ds_write_addtid_b32 v81 offset:4080\n"
    "v_pk_mul_f16 v30, v78, v74 op_sel_hi:[0,1]\n"
    "v_pk_fma_f16 v76, v1, v78, v83 op_sel:[1,1,0] op_sel_hi:[1,0,1] neg_lo:[0,1,0]\n"
    "v_pk_fma_f16 v77, v78, v74, v30 op_sel:[1,1,0] op_sel_hi:[1,0,1] neg_lo:[0,1,0]\n"
    "v_pk_mul_f16 v82, v76, v18 op_sel_hi:[0,1]\n"
    "ds_write_addtid_b32 v76 offset:6528\n"
    "ds_write_addtid_b32 v77 offset:5440\n"
    "v_pk_mul_f16 v66, v78, v87 op_sel_hi:[0,1]\n"
    "v_pk_fma_f16 v67, v76, v18, v82 op_sel:[1,1,0] op_sel_hi:[1,0,1] neg_lo:[0,1,0]\n"
    "v_pk_fma_f16 v86, v78, v87, v66 op_sel:[1,1,0] op_sel_hi:[1,0,1] neg_lo:[0,1,0]\n"
    "v_pk_mul_f16 v80, v76, v19 op_sel_hi:[0,1]\n"
    "ds_write_addtid_b32 v67 offset:6800\n"
    "ds_write_addtid_b32 v86 offset:5712\n"
    "v_pk_mul_f16 v3, v78, v0 op_sel_hi:[0,1]\n"
    "v_pk_fma_f16 v1, v76, v19, v80 op_sel:[1,1,0] op_sel_hi:[1,0,1] neg_lo:[0,1,0]\n"
    "v_pk_fma_f16 v30, v78, v0, v3 op_sel:[1,1,0] op_sel_hi:[1,0,1] neg_lo:[0,1,0]\n"
    "v_pk_mul_f16 v81, v76, v75 op_sel_hi:[0,1]\n"
    "ds_write_addtid_b32 v1 offset:7072\n"
    "ds_write_addtid_b32 v30 offset:5984\n"
    "v_pk_mul_f16 v77, v78, v79 op_sel_hi:[0,1]\n"
    "v_pk_fma_f16 v18, v76, v75, v81 op_sel:[1,1,0] op_sel_hi:[1,0,1] neg_lo:[0,1,0]\n"
    "v_pk_fma_f16 v66, v78, v79, v77 op_sel:[1,1,0] op_sel_hi:[1,0,1] neg_lo:[0,1,0]\n"
    "v_pk_mul_f16 v82, v76, v74 op_sel_hi:[0,1]\n"
    "ds_write_addtid_b32 v18 offset:7344\n"
    "ds_write_addtid_b32 v66 offset:6256\n"
    "v_pk_mul_f16 v86, v76, v87 op_sel_hi:[0,1]\n"
    "v_pk_fma_f16 v19, v76, v74, v82 op_sel:[1,1,0] op_sel_hi:[1,0,1] neg_lo:[0,1,0]\n"
    "v_pk_fma_f16 v3, v76, v87, v86 op_sel:[1,1,0] op_sel_hi:[1,0,1] neg_lo:[0,1,0]\n"
    "v_pk_mul_f16 v67, v76, v0 op_sel_hi:[0,1]\n"
    "ds_write_addtid_b32 v19 offset:7616\n"
    "ds_write_addtid_b32 v3 offset:7888\n"
    "v_pk_mul_f16 v30, v76, v79 op_sel_hi:[0,1]\n"
    "v_pk_fma_f16 v1, v76, v0, v67 op_sel:[1,1,0] op_sel_hi:[1,0,1] neg_lo:[0,1,0]\n"
    "v_pk_fma_f16 v75, v76, v79, v30 op_sel:[1,1,0] op_sel_hi:[1,0,1] neg_lo:[0,1,0]\n"
    "v_pk_mul_f32 v[80:81], v[64:65], v[64:65] op_sel_hi:[0,1]\n"
    "ds_write_addtid_b32 v1 offset:8160\n"
    "ds_write_addtid_b32 v75 offset:8432\n"
    "v_pk_fma_f32 v[82:83], v[64:65], v[64:65], v[80:81] op_sel:[1,1,0] op_sel_hi:[1,0,1] neg_lo:[0,1,0]\n"
    "v_cvt_pk_f16_f32 v30, v70, v71\n"
    "ds_read_b128 v[88:91], v31 offset:0\n"
    "v_pk_mul_f32 v[86:87], v[82:83], v[82:83] op_sel_hi:[0,1]\n"
    "v_pk_mul_f32 v[18:19], v[84:85], v[84:85] op_sel_hi:[0,1]\n"
    "ds_read_b128 v[92:95], v31 offset:32\n"
    "v_pk_fma_f32 v[66:67], v[82:83], v[82:83], v[86:87] op_sel:[1,1,0] op_sel_hi:[1,0,1] neg_lo:[0,1,0]\n"
    "v_pk_fma_f32 v[96:97], v[84:85], v[84:85], v[18:19] op_sel:[1,1,0] op_sel_hi:[1,0,1] neg_lo:[0,1,0]\n"
    "ds_read_b128 v[76:79], v31 offset:64\n"
    "v_pk_mul_f32 v[98:99], v[66:67], v[66:67] op_sel_hi:[0,1]\n"
    "v_cvt_pk_f16_f32 v74, v96, v97\n"
    "ds_read_b128 v[100:103], v31 offset:96\n"
    "v_pk_fma_f32 v[80:81], v[66:67], v[66:67], v[98:99] op_sel:[1,1,0] op_sel_hi:[1,0,1] neg_lo:[0,1,0]\n"
    "v_cvt_pk_f16_f32 v75, v72, v73\n"
    "ds_read_b128 v[104:107], v31 offset:128\n"
    "v_pk_mul_f32 v[70:71], v[80:81], v[80:81] op_sel_hi:[0,1]\n"
    "v_cvt_pk_f16_f32 v0, v80, -v81\n"
    "ds_read_b128 v[108:111], v31 offset:160\n"
    "v_pk_fma_f32 v[86:87], v[80:81], v[80:81], v[70:71] op_sel:[1,1,0] op_sel_hi:[1,0,1] neg_lo:[0,1,0]\n"
    "v_pk_mul_f32 v[18:19], v[96:97], v[96:97] op_sel_hi:[0,1]\n"
    "ds_read_b128 v[112:115], v31 offset:192\n"
    "v_cvt_pk_f16_f32 v1, v86, -v87\n"
    "v_pk_mul_f32 v[84:85], v[86:87], v[86:87] op_sel_hi:[0,1]\n"
    "ds_read_b128 v[116:119], v31 offset:224\n"
    "v_pk_mul_f16 v3, v0, v1 op_sel_hi:[0,1]\n"
    "v_pk_fma_f32 v[72:73], v[86:87], v[86:87], v[84:85] op_sel:[1,1,0] op_sel_hi:[1,0,1] neg_lo:[0,1,0]\n"
    "v_pk_fma_f16 v98, v0, v1, v3 op_sel:[1,1,0] op_sel_hi:[1,0,1] neg_lo:[0,1,0]\n"
    "v_cvt_pk_f16_f32 v99, v72, -v73\n"
    "ds_write_addtid_b32 v0 offset:272\n"
    "ds_write_addtid_b32 v1 offset:544\n"
    "v_pk_mul_f16 v70, v0, v99 op_sel_hi:[0,1]\n"
    "v_pk_mul_f16 v71, v1, v99 op_sel_hi:[0,1]\n"
    "v_pk_fma_f16 v80, v0, v99, v70 op_sel:[1,1,0] op_sel_hi:[1,0,1] neg_lo:[0,1,0]\n"
    "v_pk_fma_f16 v81, v1, v99, v71 op_sel:[1,1,0] op_sel_hi:[1,0,1] neg_lo:[0,1,0]\n"
    "ds_write_addtid_b32 v98 offset:816\n"
    "ds_write_addtid_b32 v99 offset:1088\n"
    "v_pk_mul_f32 v[120:121], v[72:73], v[72:73] op_sel_hi:[0,1]\n"
    "v_pk_mul_f16 v122, v98, v99 op_sel_hi:[0,1]\n"
    "v_pk_fma_f32 v[84:85], v[72:73], v[72:73], v[120:121] op_sel:[1,1,0] op_sel_hi:[1,0,1] neg_lo:[0,1,0]\n"
    "v_pk_fma_f16 v3, v98, v99, v122 op_sel:[1,1,0] op_sel_hi:[1,0,1] neg_lo:[0,1,0]\n"
    "ds_write_addtid_b32 v80 offset:1360\n"
    "ds_write_addtid_b32 v81 offset:1632\n"
    "v_cvt_pk_f16_f32 v86, v84, -v85\n"
    "v_pk_mul_f32 v[124:125], v[84:85], v[84:85] op_sel_hi:[0,1]\n"
    "v_pk_mul_f16 v87, v86, v0 op_sel_hi:[0,1]\n"
    "v_pk_mul_f16 v70, v86, v1 op_sel_hi:[0,1]\n"
    "ds_write_addtid_b32 v3 offset:1904\n"
    "ds_write_addtid_b32 v86 offset:2176\n"
    "v_pk_fma_f16 v71, v86, v0, v87 op_sel:[1,1,0] op_sel_hi:[1,0,1] neg_lo:[0,1,0]\n"
    "v_pk_fma_f16 v126, v86, v1, v70 op_sel:[1,1,0] op_sel_hi:[1,0,1] neg_lo:[0,1,0]\n"
    "v_pk_mul_f16 v127, v86, v98 op_sel_hi:[0,1]\n"
    "v_pk_fma_f32 v[72:73], v[84:85], v[84:85], v[124:125] op_sel:[1,1,0] op_sel_hi:[1,0,1] neg_lo:[0,1,0]\n"
    "ds_write_addtid_b32 v71 offset:2448\n"
    "ds_write_addtid_b32 v126 offset:2720\n"
    "v_pk_fma_f16 v120, v86, v98, v127 op_sel:[1,1,0] op_sel_hi:[1,0,1] neg_lo:[0,1,0]\n"
    "v_cvt_pk_f16_f32 v121, v72, -v73\n"
    "v_pk_mul_f16 v122, v86, v99 op_sel_hi:[0,1]\n"
    "v_pk_mul_f16 v123, v121, v0 op_sel_hi:[0,1]\n"
    "ds_write_addtid_b32 v120 offset:2992\n"
    "ds_write_addtid_b32 v121 offset:4352\n"
    "v_pk_fma_f16 v87, v86, v99, v122 op_sel:[1,1,0] op_sel_hi:[1,0,1] neg_lo:[0,1,0]\n"
    "v_pk_fma_f16 v70, v121, v0, v123 op_sel:[1,1,0] op_sel_hi:[1,0,1] neg_lo:[0,1,0]\n"
    "v_pk_mul_f16 v128, v86, v80 op_sel_hi:[0,1]\n"
    "v_pk_mul_f16 v84, v121, v1 op_sel_hi:[0,1]\n"
    "ds_write_addtid_b32 v87 offset:3264\n"
    "ds_write_addtid_b32 v70 offset:4624\n"
    "v_pk_fma_f16 v71, v86, v80, v128 op_sel:[1,1,0] op_sel_hi:[1,0,1] neg_lo:[0,1,0]\n"
    "v_pk_fma_f16 v85, v121, v1, v84 op_sel:[1,1,0] op_sel_hi:[1,0,1] neg_lo:[0,1,0]\n"
    "v_pk_mul_f16 v124, v86, v81 op_sel_hi:[0,1]\n"
    "v_pk_mul_f16 v125, v121, v98 op_sel_hi:[0,1]\n"
    "ds_write_addtid_b32 v71 offset:3536\n"
    "ds_write_addtid_b32 v85 offset:4896\n"
    "v_pk_fma_f16 v126, v86, v81, v124 op_sel:[1,1,0] op_sel_hi:[1,0,1] neg_lo:[0,1,0]\n"
    "v_pk_fma_f16 v127, v121, v98, v125 op_sel:[1,1,0] op_sel_hi:[1,0,1] neg_lo:[0,1,0]\n"
    "v_pk_mul_f16 v120, v86, v3 op_sel_hi:[0,1]\n"
    "v_pk_mul_f16 v122, v86, v121 op_sel_hi:[0,1]\n"
    "ds_write_addtid_b32 v126 offset:3808\n"
    "ds_write_addtid_b32 v127 offset:5168\n"
    "v_pk_fma_f16 v123, v86, v3, v120 op_sel:[1,1,0] op_sel_hi:[1,0,1] neg_lo:[0,1,0]\n"
    "v_pk_fma_f16 v70, v86, v121, v122 op_sel:[1,1,0] op_sel_hi:[1,0,1] neg_lo:[0,1,0]\n"
    "s_waitcnt vmcnt(5)\n"
    "v_lshlrev_b32 v128, 16, v12\n"
    "s_waitcnt vmcnt(4)\n"
    "v_lshlrev_b32 v129, 16, v14\n"
    "v_and_b32 v130, 0xffff0000, v12\n"
    "v_and_b32 v131, 0xffff0000, v14\n"
    "ds_write_addtid_b32 v123 offset:4080\n"
    "ds_write_addtid_b32 v70 offset:6528\n"
    "s_waitcnt vmcnt(0)\n"
    "s_cmp_eq_u32 s9, 0\n"
    "s_cbranch_scc0 .LqbB%=\n"
    "v_mul_f32 v6, 0x3fb8aa3b, v6\n"
    "v_exp_f32 v6, v6\n"
    "s_nop 0\n"
    "v_cvt_pk_bf16_f32 v6, 0, v6\n"
    "s_branch .LqjB%=\n"
    ".LqbB%=:\n"
    "v_lshlrev_b32 v84, 16, v15\n"
    "v_lshlrev_b32 v85, 16, v32\n"
    "v_and_b32 v124, 0xffff0000, v15\n"
    "v_and_b32 v125, 0xffff0000, v32\n"
    "v_pk_mul_f32 v[136:137], v[8:9], v[10:11] op_sel_hi:[0,1]\n"
    "v_pk_mul_f32 v[132:133], v[128:129], v[84:85] op_sel_hi:[0,1]\n"
    "v_pk_mul_f32 v[134:135], v[130:131], v[124:125] op_sel_hi:[0,1]\n"
    "v_pk_fma_f32 v[8:9], v[8:9], v[10:11], v[136:137] op_sel:[1,1,0] op_sel_hi:[1,0,1] neg_lo:[0,1,0]\n"
    "v_pk_fma_f32 v[128:129], v[128:129], v[84:85], v[132:133] op_sel:[1,1,0] op_sel_hi:[1,0,1] neg_lo:[0,1,0]\n"
    "v_pk_fma_f32 v[130:131], v[130:131], v[124:125], v[134:135] op_sel:[1,1,0] op_sel_hi:[1,0,1] neg_lo:[0,1,0]\n"
    ".LqjB%=:\n"
    "v_pk_mul_f16 v71, v121, v99 op_sel_hi:[0,1]\n"
    "v_pk_mul_f16 v87, v70, v0 op_sel_hi:[0,1]\n"
    "v_pk_fma_f16 v126, v121, v99, v71 op_sel:[1,1,0] op_sel_hi:[1,0,1] neg_lo:[0,1,0]\n"
    "v_pk_fma_f16 v120, v70, v0, v87 op_sel:[1,1,0] op_sel_hi:[1,0,1] neg_lo:[0,1,0]\n"
    "ds_write_addtid_b32 v126 offset:5440\n"
    "ds_write_addtid_b32 v120 offset:6800\n"
    "v_pk_mul_f16 v122, v121, v80 op_sel_hi:[0,1]\n"
    "v_pk_mul_f16 v12, v70, v1 op_sel_hi:[0,1]\n"
    "v_pk_fma_f16 v14, v121, v80, v122 op_sel:[1,1,0] op_sel_hi:[1,0,1] neg_lo:[0,1,0]\n"
    "v_pk_fma_f16 v123, v70, v1, v12 op_sel:[1,1,0] op_sel_hi:[1,0,1] neg_lo:[0,1,0]\n"
    "ds_write_addtid_b32 v14 offset:5712\n"
    "ds_write_addtid_b32 v123 offset:7072\n"
    "v_pk_mul_f16 v32, v121, v81 op_sel_hi:[0,1]\n"
    "v_pk_mul_f16 v10, v70, v98 op_sel_hi:[0,1]\n"
    "v_pk_fma_f16 v11, v121, v81, v32 op_sel:[1,1,0] op_sel_hi:[1,0,1] neg_lo:[0,1,0]\n"
    "v_pk_fma_f16 v71, v70, v98, v10 op_sel:[1,1,0] op_sel_hi:[1,0,1] neg_lo:[0,1,0]\n"
    "ds_write_addtid_b32 v11 offset:5984\n"
    "ds_write_addtid_b32 v71 offset:7344\n"
    "v_pk_mul_f16 v0, v121, v3 op_sel_hi:[0,1]\n"
    "v_pk_mul_f16 v15, v70, v99 op_sel_hi:[0,1]\n"
    "v_pk_fma_f16 v120, v121, v3, v0 op_sel:[1,1,0] op_sel_hi:[1,0,1] neg_lo:[0,1,0]\n"
    "v_pk_fma_f16 v1, v70, v99, v15 op_sel:[1,1,0] op_sel_hi:[1,0,1] neg_lo:[0,1,0]\n"
    "ds_write_addtid_b32 v120 offset:6256\n"
    "ds_write_addtid_b32 v1 offset:7616\n"
    "v_pk_mul_f16 v12, v70, v80 op_sel_hi:[0,1]\n"
    "v_mul_f32 v6, v4, v6\n"
    "v_pk_mul_f16 v32, v70, v81 op_sel_hi:[0,1]\n"
    "v_pk_fma_f16 v10, v70, v80, v12 op_sel:[1,1,0] op_sel_hi:[1,0,1] neg_lo:[0,1,0]\n"
    "v_max3_f32 v11, v34, v35, v6\n"
    "v_pk_fma_f16 v71, v70, v81, v32 op_sel:[1,1,0] op_sel_hi:[1,0,1] neg_lo:[0,1,0]\n"
    "ds_write_addtid_b32 v10 offset:7888\n"
    "ds_write_addtid_b32 v71 offset:8160\n"
    "v_max3_f32 v0, abs(v128), abs(v129), abs(v130)\n"
    "v_mul_f32 v11, 0x45000000, v11\n"
    "v_pk_mul_f16 v98, v70, v3 op_sel_hi:[0,1]\n"
    "v_max3_f32 v0, abs(v131), abs(v8), v0\n"
    "v_med3_f32 v11, v11, 0, s48\n"
    "v_pk_fma_f16 v1, v70, v3, v98 op_sel:[1,1,0] op_sel_hi:[1,0,1] neg_lo:[0,1,0]\n"
    "v_max_f32_e64 v0, abs(v9), v0\n"
    "v_ceil_f32 v11, v11\n"
    "ds_write_addtid_b32 v1 offset:8432\n"
    "v_lshrrev_b32 v0, 23, v0\n"
    "v_cvt_u32_f32 v11, v11\n"
    "ds_read_b128 v[84:87], v31 offset:0\n"
    "v_add_u32 v0, v0, v11\n"
    "v_pk_fma_f32 v[80:81], v[96:97], v[96:97], v[18:19] op_sel:[1,1,0] op_sel_hi:[1,0,1] neg_lo:[0,1,0]\n"
    "ds_read_b128 v[120:123], v31 offset:32\n"
    "v_max_u32_dpp v0, v0, v0 row_ror:8 row_mask:0xf bank_mask:0xf\n"
    "v_cvt_pk_f16_f32 v4, v80, v81\n"
    "ds_read_b128 v[124:127], v31 offset:64\n"
    "v_max_u32_dpp v0, v0, v0 row_ror:4 row_mask:0xf bank_mask:0xf\n"
    "v_cvt_pk_f16_f32 v3, v68, v69\n"
    "ds_read_b128 v[132:135], v31 offset:96\n"
    "v_max_u32_dpp v0, v0, v0 row_ror:2 row_mask:0xf bank_mask:0xf\n"
    "v_cvt_pk_f16_f32 v1, v64, v65\n"
    "ds_read_b128 v[136:139], v31 offset:128\n"
    "v_max_u32_dpp v0, v0, v0 row_ror:1 row_mask:0xf bank_mask:0xf\n"
    "v_pk_mul_f32 v[10:11], v[72:73], v[72:73] op_sel_hi:[0,1]\n"
    "v_readlane_b32 s50, v0, 0\n"
    "v_readlane_b32 s51, v0, 16\n"
    "v_readlane_b32 s52, v0, 32\n"
    "v_readlane_b32 s53, v0, 48\n"
    "ds_read_b128 v[96:99], v31 offset:160\n"
    "s_max_u32 s50, s50, s51\n"
    "s_max_u32 s52, s52, s53\n"
    "s_max_u32 s50, s50, s52\n"
    "s_max_u32 s50, s50, 14\n"
    "s_min_u32 s50, s50, 266\n"
    "s_sub_u32 s51, 267, s50\n"
    "s_lshl_b32 s51, s51, 23\n"
    "s_sub_u32 s52, s50, s15\n"
    "s_lshl_b32 s52, s52, 23\n"
    "s_mov_b32 s50, s51\n"
    "v_pk_mul_f32 v[128:129], v[128:129], s[50:51]\n"
    "ds_read_b128 v[68:71], v31 offset:192\n"
    "v_cvt_pk_f16_f32 v18, v128, v129\n"
    "v_pk_mul_f32 v[130:131], v[130:131], s[50:51]\n"
    "ds_read_b128 v[140:143], v31 offset:224\n"
    "v_pk_mul_f16 v19, v18, v30 op_sel_hi:[0,1]\n"
    "v_pk_mul_f16 v64, v18, v74 op_sel_hi:[0,1]\n"
    "v_pk_fma_f16 v65, v18, v30, v19 op_sel:[1,1,0] op_sel_hi:[1,0,1] neg_lo:[0,1,0]\n"
    "v_pk_mul_f16 v80, v18, v75 op_sel_hi:[0,1]\n"
    "ds_write_addtid_b32 v18 offset:272\n"
    "ds_write_addtid_b32 v65 offset:544\n"
    "v_pk_fma_f16 v0, v18, v74, v64 op_sel:[1,1,0] op_sel_hi:[1,0,1] neg_lo:[0,1,0]\n"
    "v_pk_fma_f16 v81, v18, v75, v80 op_sel:[1,1,0] op_sel_hi:[1,0,1] neg_lo:[0,1,0]\n"
    "v_pk_mul_f16 v12, v18, v4 op_sel_hi:[0,1]\n"
    "v_pk_mul_f16 v14, v65, v75 op_sel_hi:[0,1]\n"
    "ds_write_addtid_b32 v0 offset:1360\n"
    "ds_write_addtid_b32 v81 offset:816\n"
    "v_pk_fma_f16 v15, v18, v4, v12 op_sel:[1,1,0] op_sel_hi:[1,0,1] neg_lo:[0,1,0]\n"
    "v_pk_fma_f16 v128, v65, v75, v14 op_sel:[1,1,0] op_sel_hi:[1,0,1] neg_lo:[0,1,0]\n"
    "v_pk_mul_f16 v19, v0, v4 op_sel_hi:[0,1]\n"
    "v_pk_mul_f16 v30, v18, v3 op_sel_hi:[0,1]\n"
    "ds_write_addtid_b32 v15 offset:5712\n"
    "ds_write_addtid_b32 v128 offset:1088\n"
    "v_pk_fma_f16 v64, v0, v4, v19 op_sel:[1,1,0] op_sel_hi:[1,0,1] neg_lo:[0,1,0]\n"
    "v_pk_fma_f16 v74, v18, v3, v30 op_sel:[1,1,0] op_sel_hi:[1,0,1] neg_lo:[0,1,0]\n"
    "v_pk_mul_f16 v80, v15, v4 op_sel_hi:[0,1]\n"
    "v_pk_mul_f16 v129, v65, v3 op_sel_hi:[0,1]\n"
    "ds_write_addtid_b32 v64 offset:6256\n"
    "ds_write_addtid_b32 v74 offset:2448\n"
    "v_pk_fma_f16 v12, v15, v4, v80 op_sel:[1,1,0] op_sel_hi:[1,0,1] neg_lo:[0,1,0]\n"
    "v_pk_fma_f16 v14, v65, v3, v129 op_sel:[1,1,0] op_sel_hi:[1,0,1] neg_lo:[0,1,0]\n"
    "v_pk_mul_f16 v32, v64, v4 op_sel_hi:[0,1]\n"
    "v_pk_mul_f16 v34, v81, v3 op_sel_hi:[0,1]\n"
    "ds_write_addtid_b32 v12 offset:4624\n"
    "ds_write_addtid_b32 v14 offset:2720\n"
    "v_pk_fma_f16 v35, v64, v4, v32 op_sel:[1,1,0] op_sel_hi:[1,0,1] neg_lo:[0,1,0]\n"
    "v_pk_fma_f16 v30, v81, v3, v34 op_sel:[1,1,0] op_sel_hi:[1,0,1] neg_lo:[0,1,0]\n"
    "v_pk_mul_f16 v18, v12, v4 op_sel_hi:[0,1]\n"
    "v_pk_mul_f16 v19, v128, v3 op_sel_hi:[0,1]\n"
    "ds_write_addtid_b32 v35 offset:5168\n"
    "ds_write_addtid_b32 v30 offset:2992\n"
    "v_pk_fma_f16 v74, v12, v4, v18 op_sel:[1,1,0] op_sel_hi:[1,0,1] neg_lo:[0,1,0]\n"
    "v_pk_fma_f16 v65, v128, v3, v19 op_sel:[1,1,0] op_sel_hi:[1,0,1] neg_lo:[0,1,0]\n"
    "s_waitcnt lgkmcnt(0)\n"
    "s_load_dword s37, s[32:33], s58\n"
    "v_pk_mul_f16 v80, v35, v4 op_sel_hi:[0,1]\n"
    "ds_write_addtid_b32 v74 offset:4896\n"
    "ds_write_addtid_b32 v65 offset:3264\n"
    "v_pk_mul_f16 v14, v0, v75 op_sel_hi:[0,1]\n"
    "v_pk_fma_f16 v81, v35, v4, v80 op_sel:[1,1,0] op_sel_hi:[1,0,1] neg_lo:[0,1,0]\n"
    "v_pk_fma_f16 v129, v0, v75, v14 op_sel:[1,1,0] op_sel_hi:[1,0,1] neg_lo:[0,1,0]\n"
    "v_pk_mul_f16 v32, v74, v4 op_sel_hi:[0,1]\n"
    "ds_write_addtid_b32 v81 offset:5440\n"
    "ds_write_addtid_b32 v129 offset:1632\n"
    "v_pk_mul_f16 v30, v0, v3 op_sel_hi:[0,1]\n"
    "v_pk_fma_f16 v128, v74, v4, v32 op_sel:[1,1,0] op_sel_hi:[1,0,1] neg_lo:[0,1,0]\n"
    "v_pk_fma_f16 v12, v0, v3, v30 op_sel:[1,1,0] op_sel_hi:[1,0,1] neg_lo:[0,1,0]\n"
    "v_pk_mul_f16 v18, v129, v3 op_sel_hi:[0,1]\n"
    "ds_write_addtid_b32 v128 offset:3536\n"
    "ds_write_addtid_b32 v12 offset:1904\n"
    "v_pk_mul_f16 v19, v15, v3 op_sel_hi:[0,1]\n"
    "v_pk_fma_f16 v65, v129, v3, v18 op_sel:[1,1,0] op_sel_hi:[1,0,1] neg_lo:[0,1,0]\n"
    "v_pk_fma_f16 v14, v15, v3, v19 op_sel:[1,1,0] op_sel_hi:[1,0,1] neg_lo:[0,1,0]\n"
    "v_pk_mul_f16 v75, v64, v3 op_sel_hi:[0,1]\n"
    "ds_write_addtid_b32 v65 offset:2176\n"
    "ds_write_addtid_b32 v14 offset:5984\n"
    "v_pk_fma_f16 v80, v64, v3, v75 op_sel:[1,1,0] op_sel_hi:[1,0,1] neg_lo:[0,1,0]\n"
    "v_cvt_pk_f16_f32 v4, v130, v131\n"
    "ds_write_addtid_b32 v80 offset:6528\n"
    "v_pk_mul_f16 v30, v4, v1 op_sel_hi:[0,1]\n"
    "v_pk_fma_f32 v[34:35], v[72:73], v[72:73], v[10:11] op_sel:[1,1,0] op_sel_hi:[1,0,1] neg_lo:[0,1,0]\n"
    "ds_read_b128 v[144:147], v31 offset:272\n"
    "v_pk_fma_f16 v32, v4, v1, v30 op_sel:[1,1,0] op_sel_hi:[1,0,1] neg_lo:[0,1,0]\n"
    "v_cvt_pk_f16_f32 v18, v34, v35\n"
    "ds_read_b128 v[148:151], v31 offset:304\n"
    "v_cvt_pk_f16_f32 v19, v82, v83\n"
    "v_pk_mul_f32 v[128:129], v[34:35], v[34:35] op_sel_hi:[0,1]\n"
    "ds_read_b128 v[152:155], v31 offset:336\n"
    "v_pk_mul_f16 v64, v4, v18 op_sel_hi:[0,1]\n"
    "v_pk_mul_f16 v65, v4, v19 op_sel_hi:[0,1]\n"
    "ds_read_b128 v[156:159], v31 offset:368\n"
    "v_pk_fma_f32 v[130:131], v[34:35], v[34:35], v[128:129] op_sel:[1,1,0] op_sel_hi:[1,0,1] neg_lo:[0,1,0]\n"
    "v_pk_fma_f16 v10, v4, v18, v64 op_sel:[1,1,0] op_sel_hi:[1,0,1] neg_lo:[0,1,0]\n"
    "ds_read_b128 v[72:75], v31 offset:400\n"
    "v_pk_fma_f16 v11, v4, v19, v65 op_sel:[1,1,0] op_sel_hi:[1,0,1] neg_lo:[0,1,0]\n"
    "v_cvt_pk_f16_f32 v30, v130, v131\n"
    "ds_read_b128 v[160:163], v31 offset:432\n"
    "v_pk_mul_f16 v0, v4, v30 op_sel_hi:[0,1]\n"
    "v_pk_mul_f16 v1, v32, v19 op_sel_hi:[0,1]\n"
    "ds_read_b128 v[80:83], v31 offset:464\n"
    "v_pk_fma_f16 v3, v4, v30, v0 op_sel:[1,1,0] op_sel_hi:[1,0,1] neg_lo:[0,1,0]\n"
    "v_pk_fma_f16 v12, v32, v19, v1 op_sel:[1,1,0] op_sel_hi:[1,0,1] neg_lo:[0,1,0]\n"
    "ds_read_b128 v[164:167], v31 offset:496\n"
    "v_cvt_pk_f16_f32 v14, v66, v67\n"
    "v_pk_mul_f16 v15, v10, v30 op_sel_hi:[0,1]\n"
    "v_pk_mul_f16 v18, v4, v14 op_sel_hi:[0,1]\n"
    "v_pk_fma_f16 v34, v10, v30, v15 op_sel:[1,1,0] op_sel_hi:[1,0,1] neg_lo:[0,1,0]\n"
    "ds_write_addtid_b32 v4 offset:272\n"
    "ds_write_addtid_b32 v32 offset:544\n"
    "v_pk_fma_f16 v35, v4, v14, v18 op_sel:[1,1,0] op_sel_hi:[1,0,1] neg_lo:[0,1,0]\n"
    "v_pk_mul_f16 v64, v3, v30 op_sel_hi:[0,1]\n"
    "v_pk_mul_f16 v65, v32, v14 op_sel_hi:[0,1]\n"
    "v_pk_fma_f16 v0, v3, v30, v64 op_sel:[1,1,0] op_sel_hi:[1,0,1] neg_lo:[0,1,0]\n"
    "ds_write_addtid_b32 v10 offset:1360\n"
    "ds_write_addtid_b32 v11 offset:816\n"
    "v_pk_fma_f16 v1, v32, v14, v65 op_sel:[1,1,0] op_sel_hi:[1,0,1] neg_lo:[0,1,0]\n"
    "s_waitcnt lgkmcnt(11)\n"
    "v_mfma_f32_32x32x16_f16 v[168:183], v[144:147], v[88:91], 0\n"
    "v_pk_mul_f16 v66, v34, v30 op_sel_hi:[0,1]\n"
    "v_pk_mul_f16 v15, v11, v14 op_sel_hi:[0,1]\n"
    "ds_write_addtid_b32 v3 offset:5712\n"
    "ds_write_addtid_b32 v12 offset:1088\n"
    "v_pk_fma_f16 v4, v34, v30, v66 op_sel:[1,1,0] op_sel_hi:[1,0,1] neg_lo:[0,1,0]\n"
    "v_pk_fma_f16 v18, v11, v14, v15 op_sel:[1,1,0] op_sel_hi:[1,0,1] neg_lo:[0,1,0]\n"
    "s_waitcnt lgkmcnt(12)\n"
    "v_mfma_f32_32x32x16_f16 v[168:183], v[148:151], v[92:95], v[168:183]\n"
    "s_setprio 2\n"
    "v_pk_mul_f16 v64, v0, v30 op_sel_hi:[0,1]\n"
    "ds_write_addtid_b32 v34 offset:6256\n"
    "ds_write_addtid_b32 v35 offset:2448\n"
    "v_pk_mul_f16 v32, v12, v14 op_sel_hi:[0,1]\n"
    "v_pk_fma_f16 v65, v0, v30, v64 op_sel:[1,1,0] op_sel_hi:[1,0,1] neg_lo:[0,1,0]\n"
    "v_pk_fma_f16 v67, v12, v14, v32 op_sel:[1,1,0] op_sel_hi:[1,0,1] neg_lo:[0,1,0]\n"
    "s_waitcnt lgkmcnt(13)\n"
    "v_mfma_f32_32x32x16_f16 v[168:183], v[152:155], v[76:79], v[168:183]\n"
    "ds_write_addtid_b32 v0 offset:4624\n"
    "ds_write_addtid_b32 v1 offset:2720\n"
    "v_pk_mul_f16 v11, v4, v30 op_sel_hi:[0,1]\n"
    "v_pk_mul_f16 v15, v10, v19 op_sel_hi:[0,1]\n"
    "v_pk_fma_f16 v66, v4, v30, v11 op_sel:[1,1,0] op_sel_hi:[1,0,1] neg_lo:[0,1,0]\n"
    "v_pk_fma_f16 v88, v10, v19, v15 op_sel:[1,1,0] op_sel_hi:[1,0,1] neg_lo:[0,1,0]\n"
    "ds_write_addtid_b32 v4 offset:5168\n"
    "ds_write_addtid_b32 v18 offset:2992\n"
    "s_waitcnt lgkmcnt(15)\n"
    "v_mfma_f32_32x32x16_f16 v[168:183], v[156:159], v[100:103], v[168:183]\n"
    "v_pk_mul_f16 v64, v65, v30 op_sel_hi:[0,1]\n"
    "v_pk_mul_f16 v12, v10, v14 op_sel_hi:[0,1]\n"
    "v_pk_fma_f16 v32, v65, v30, v64 op_sel:[1,1,0] op_sel_hi:[1,0,1] neg_lo:[0,1,0]\n"
    "ds_write_addtid_b32 v65 offset:4896\n"
    "ds_write_addtid_b32 v67 offset:3264\n"
    "v_pk_fma_f16 v35, v10, v14, v12 op_sel:[1,1,0] op_sel_hi:[1,0,1] neg_lo:[0,1,0]\n"
    "v_mfma_f32_32x32x16_f16 v[168:183], v[72:75], v[104:107], v[168:183]\n"
    "v_pk_mul_f16 v0, v88, v14 op_sel_hi:[0,1]\n"
    "v_pk_mul_f16 v1, v3, v14 op_sel_hi:[0,1]\n"
    "ds_write_addtid_b32 v66 offset:5440\n"
    "ds_write_addtid_b32 v88 offset:1632\n"
    "v_pk_fma_f16 v4, v88, v14, v0 op_sel:[1,1,0] op_sel_hi:[1,0,1] neg_lo:[0,1,0]\n"
    "v_pk_fma_f16 v11, v3, v14, v1 op_sel:[1,1,0] op_sel_hi:[1,0,1] neg_lo:[0,1,0]\n"
    "s_waitcnt lgkmcnt(15)\n"
    "v_mfma_f32_32x32x16_f16 v[168:183], v[160:163], v[108:111], v[168:183]\n"
    "v_pk_mul_f16 v30, v34, v14 op_sel_hi:[0,1]\n"
    "ds_write_addtid_b32 v32 offset:3536\n"
    "ds_write_addtid_b32 v35 offset:1904\n"
    "v_pk_fma_f16 v10, v34, v14, v30 op_sel:[1,1,0] op_sel_hi:[1,0,1] neg_lo:[0,1,0]\n"
    "v_mul_f32 v7, v5, v7\n"
    "v_exp_f32 v18, v6\n"
    "v_mfma_f32_32x32x16_f16 v[168:183], v[80:83], v[112:115], v[168:183]\n"
    "ds_write_addtid_b32 v4 offset:2176\n"
    "ds_write_addtid_b32 v11 offset:5984\n"
    "v_fract_f32 v19, v7\n"
    "v_mul_f32 v33, s52, v16\n"
    "ds_write_addtid_b32 v10 offset:6528\n"
    "v_cos_f32 v0, v19\n"
    "v_mfma_f32_32x32x16_f16 v[168:183], v[164:167], v[116:119], v[168:183]\n"
    "ds_read_b128 v[64:67], v31 offset:272\n"
    "v_sin_f32 v1, v19\n"
    "v_mul_f32 v14, v18, v0\n"
    "ds_read_b128 v[72:75], v31 offset:304\n"
    "v_mul_f32 v15, v18, v1\n"
    "ds_read_b128 v[76:79], v31 offset:336\n"
    "v_pk_mul_f32 v[34:35], v[14:15], v[14:15] op_sel_hi:[0,1]\n"
    "v_pk_mul_f32 v[8:9], v[8:9], s[50:51]\n"
    "ds_read_b128 v[4:7], v31 offset:368\n"
    "v_pk_fma_f32 v[10:11], v[14:15], v[14:15], v[34:35] op_sel:[1,1,0] op_sel_hi:[1,0,1] neg_lo:[0,1,0]\n"
    "v_cvt_pk_f16_f32 v12, v14, v15\n"
    "v_pk_mul_f32 v[80:81], v[10:11], v[10:11] op_sel_hi:[0,1]\n"
    "ds_read_b128 v[88:91], v31 offset:400\n"
    "v_pk_fma_f32 v[82:83], v[10:11], v[10:11], v[80:81] op_sel:[1,1,0] op_sel_hi:[1,0,1] neg_lo:[0,1,0]\n"
    "v_cvt_pk_f16_f32 v30, v8, v9\n"
    "ds_read_b128 v[92:95], v31 offset:432\n"
    "v_pk_mul_f32 v[0:1], v[82:83], v[82:83] op_sel_hi:[0,1]\n"
    "v_pk_mul_f16 v3, v30, v12 op_sel_hi:[0,1]\n"
    "v_pk_fma_f32 v[18:19], v[82:83], v[82:83], v[0:1] op_sel:[1,1,0] op_sel_hi:[1,0,1] neg_lo:[0,1,0]\n"
    "ds_read_b128 v[100:103], v31 offset:464\n"
    "v_pk_mul_f32 v[104:105], v[18:19], v[18:19] op_sel_hi:[0,1]\n"
    "v_cvt_pk_f16_f32 v16, v18, -v19\n"
    "ds_read_b128 v[108:111], v31 offset:496\n"
    "v_pk_fma_f32 v[14:15], v[18:19], v[18:19], v[104:105] op_sel:[1,1,0] op_sel_hi:[1,0,1] neg_lo:[0,1,0]\n"
    "v_pk_fma_f16 v32, v30, v12, v3 op_sel:[1,1,0] op_sel_hi:[1,0,1] neg_lo:[0,1,0]\n"
    "v_cvt_pk_f16_f32 v34, v14, -v15\n"
    "v_pk_mul_f32 v[8:9], v[14:15], v[14:15] op_sel_hi:[0,1]\n"
    "ds_write_addtid_b32 v16 offset:272\n"
    "ds_write_addtid_b32 v34 offset:544\n"
    "v_pk_mul_f16 v35, v16, v34 op_sel_hi:[0,1]\n"
    "v_pk_fma_f32 v[80:81], v[14:15], v[14:15], v[8:9] op_sel:[1,1,0] op_sel_hi:[1,0,1] neg_lo:[0,1,0]\n"
    "v_pk_fma_f16 v0, v16, v34, v35 op_sel:[1,1,0] op_sel_hi:[1,0,1] neg_lo:[0,1,0]\n"
    "v_cvt_pk_f16_f32 v1, v80, -v81\n"
    "ds_write_addtid_b32 v0 offset:816\n"
    "ds_write_addtid_b32 v1 offset:1088\n"
    "v_pk_mul_f16 v18, v16, v1 op_sel_hi:[0,1]\n"
    "v_pk_mul_f16 v3, v34, v1 op_sel_hi:[0,1]\n"
    "v_pk_fma_f16 v19, v16, v1, v18 op_sel:[1,1,0] op_sel_hi:[1,0,1] neg_lo:[0,1,0]\n"
    "v_pk_fma_f16 v12, v34, v1, v3 op_sel:[1,1,0] op_sel_hi:[1,0,1] neg_lo:[0,1,0]\n"
    "ds_write_addtid_b32 v19 offset:1360\n"
    "ds_write_addtid_b32 v12 offset:1632\n"
    "v_pk_mul_f32 v[104:105], v[80:81], v[80:81] op_sel_hi:[0,1]\n"
    "v_pk_mul_f16 v8, v0, v1 op_sel_hi:[0,1]\n"
    "v_pk_fma_f32 v[14:15], v[80:81], v[80:81], v[104:105] op_sel:[1,1,0] op_sel_hi:[1,0,1] neg_lo:[0,1,0]\n"
    "v_pk_fma_f16 v9, v0, v1, v8 op_sel:[1,1,0] op_sel_hi:[1,0,1] neg_lo:[0,1,0]\n"
    "v_cvt_pk_f16_f32 v35, v14, -v15\n"
    "ds_write_addtid_b32 v9 offset:1904\n"
    "ds_write_addtid_b32 v35 offset:2176\n"
    "v_pk_mul_f16 v106, v35, v16 op_sel_hi:[0,1]\n"
    "v_pk_mul_f16 v18, v35, v34 op_sel_hi:[0,1]\n"
    "v_pk_fma_f16 v3, v35, v16, v106 op_sel:[1,1,0] op_sel_hi:[1,0,1] neg_lo:[0,1,0]\n"
    "v_pk_fma_f16 v107, v35, v34, v18 op_sel:[1,1,0] op_sel_hi:[1,0,1] neg_lo:[0,1,0]\n"
    "ds_write_addtid_b32 v3 offset:2448\n"
    "ds_write_addtid_b32 v107 offset:2720\n"
    "v_pk_mul_f32 v[112:113], v[14:15], v[14:15] op_sel_hi:[0,1]\n"
    "v_pk_mul_f16 v80, v35, v0 op_sel_hi:[0,1]\n"
    "v_pk_fma_f32 v[114:115], v[14:15], v[14:15], v[112:113] op_sel:[1,1,0] op_sel_hi:[1,0,1] neg_lo:[0,1,0]\n"
    "v_pk_fma_f16 v8, v35, v0, v80 op_sel:[1,1,0] op_sel_hi:[1,0,1] neg_lo:[0,1,0]\n"
    "v_cvt_pk_f16_f32 v81, v114, -v115\n"
    "ds_write_addtid_b32 v8 offset:2992\n"
    "ds_write_addtid_b32 v81 offset:4352\n"
    "v_pk_mul_f16 v104, v35, v1 op_sel_hi:[0,1]\n"
    "v_pk_mul_f16 v105, v81, v16 op_sel_hi:[0,1]\n"
    "v_pk_fma_f16 v18, v35, v1, v104 op_sel:[1,1,0] op_sel_hi:[1,0,1] neg_lo:[0,1,0]\n"
    "v_pk_fma_f16 v3, v81, v16, v105 op_sel:[1,1,0] op_sel_hi:[1,0,1] neg_lo:[0,1,0]\n"
    "ds_write_addtid_b32 v18 offset:3264\n"
    "ds_write_addtid_b32 v3 offset:4624\n"
    "s_waitcnt lgkmcnt(15)\n"
    "v_mfma_f32_32x32x16_f16 v[168:183], v[64:67], v[84:87], v[168:183]\n"
    "v_pk_mul_f16 v14, v35, v19 op_sel_hi:[0,1]\n"
    "v_pk_mul_f16 v15, v81, v34 op_sel_hi:[0,1]\n"
    "v_pk_fma_f16 v80, v35, v19, v14 op_sel:[1,1,0] op_sel_hi:[1,0,1] neg_lo:[0,1,0]\n"
    "v_pk_fma_f16 v8, v81, v34, v15 op_sel:[1,1,0] op_sel_hi:[1,0,1] neg_lo:[0,1,0]\n"
    "ds_write_addtid_b32 v80 offset:3536\n"
    "ds_write_addtid_b32 v8 offset:4896\n"
    "v_mfma_f32_32x32x16_f16 v[168:183], v[72:75], v[120:123], v[168:183]\n"
    "v_pk_mul_f16 v112, v35, v12 op_sel_hi:[0,1]\n"
    "v_pk_mul_f16 v113, v81, v0 op_sel_hi:[0,1]\n"
    "v_pk_fma_f16 v18, v35, v12, v112 op_sel:[1,1,0] op_sel_hi:[1,0,1] neg_lo:[0,1,0]\n"
    "v_pk_fma_f16 v3, v81, v0, v113 op_sel:[1,1,0] op_sel_hi:[1,0,1] neg_lo:[0,1,0]\n"
    "ds_write_addtid_b32 v18 offset:3808\n"
    "ds_write_addtid_b32 v3 offset:5168\n"
    "v_mfma_f32_32x32x16_f16 v[168:183], v[76:79], v[124:127], v[168:183]\n"
    "v_pk_mul_f16 v14, v35, v9 op_sel_hi:[0,1]\n"
    "v_pk_mul_f16 v15, v35, v81 op_sel_hi:[0,1]\n"
    "v_pk_fma_f16 v80, v35, v9, v14 op_sel:[1,1,0] op_sel_hi:[1,0,1] neg_lo:[0,1,0]\n"
    "v_pk_fma_f16 v8, v35, v81, v15 op_sel:[1,1,0] op_sel_hi:[1,0,1] neg_lo:[0,1,0]\n"
    "ds_write_addtid_b32 v80 offset:4080\n"
    "ds_write_addtid_b32 v8 offset:6528\n"
    "v_mfma_f32_32x32x16_f16 v[168:183], v[4:7], v[132:135], v[168:183]\n"
    "v_pk_mul_f16 v112, v81, v1 op_sel_hi:[0,1]\n"
    "v_pk_mul_f16 v113, v8, v16 op_sel_hi:[0,1]\n"
    "v_pk_fma_f16 v18, v81, v1, v112 op_sel:[1,1,0] op_sel_hi:[1,0,1] neg_lo:[0,1,0]\n"
    "v_pk_fma_f16 v3, v8, v16, v113 op_sel:[1,1,0] op_sel_hi:[1,0,1] neg_lo:[0,1,0]\n"
    "ds_write_addtid_b32 v18 offset:5440\n"
    "ds_write_addtid_b32 v3 offset:6800\n"
    "v_mfma_f32_32x32x16_f16 v[168:183], v[88:91], v[136:139], v[168:183]\n"
    "v_pk_mul_f16 v14, v81, v19 op_sel_hi:[0,1]\n"
    "v_pk_mul_f16 v15, v8, v34 op_sel_hi:[0,1]\n"
    "v_pk_fma_f16 v35, v81, v19, v14 op_sel:[1,1,0] op_sel_hi:[1,0,1] neg_lo:[0,1,0]\n"
    "v_pk_fma_f16 v80, v8, v34, v15 op_sel:[1,1,0] op_sel_hi:[1,0,1] neg_lo:[0,1,0]\n"
    "ds_write_addtid_b32 v35 offset:5712\n"
    "ds_write_addtid_b32 v80 offset:7072\n"
    "v_mfma_f32_32x32x16_f16 v[168:183], v[92:95], v[96:99], v[168:183]\n"
    "v_pk_mul_f16 v112, v81, v12 op_sel_hi:[0,1]\n"
    "v_pk_mul_f16 v113, v8, v0 op_sel_hi:[0,1]\n"
    "v_pk_fma_f16 v16, v81, v12, v112 op_sel:[1,1,0] op_sel_hi:[1,0,1] neg_lo:[0,1,0]\n"
    "v_pk_fma_f16 v3, v8, v0, v113 op_sel:[1,1,0] op_sel_hi:[1,0,1] neg_lo:[0,1,0]\n"
    "ds_write_addtid_b32 v16 offset:5984\n"
    "ds_write_addtid_b32 v3 offset:7344\n"
    "v_mfma_f32_32x32x16_f16 v[168:183], v[100:103], v[68:71], v[168:183]\n"
    "v_pk_mul_f16 v14, v81, v9 op_sel_hi:[0,1]\n"
    "v_pk_mul_f16 v15, v8, v1 op_sel_hi:[0,1]\n"
    "v_pk_fma_f16 v18, v81, v9, v14 op_sel:[1,1,0] op_sel_hi:[1,0,1] neg_lo:[0,1,0]\n"
    "v_pk_fma_f16 v34, v8, v1, v15 op_sel:[1,1,0] op_sel_hi:[1,0,1] neg_lo:[0,1,0]\n"
    "ds_write_addtid_b32 v18 offset:6256\n"
    "ds_write_addtid_b32 v34 offset:7616\n"
    "s_waitcnt lgkmcnt(15)\n"
    "v_mfma_f32_32x32x16_f16 v[168:183], v[108:111], v[140:143], v[168:183]\n"
    "v_pk_mul_f16 v35, v8, v19 op_sel_hi:[0,1]\n"
    "v_pk_mul_f16 v80, v8, v12 op_sel_hi:[0,1]\n"
    "v_pk_fma_f16 v16, v8, v19, v35 op_sel:[1,1,0] op_sel_hi:[1,0,1] neg_lo:[0,1,0]\n"
    "v_pk_fma_f16 v112, v8, v12, v80 op_sel:[1,1,0] op_sel_hi:[1,0,1] neg_lo:[0,1,0]\n"
    "ds_write_addtid_b32 v16 offset:7888\n"
    "ds_write_addtid_b32 v112 offset:8160\n"
    "v_pk_mul_f16 v113, v8, v9 op_sel_hi:[0,1]\n"
    "v_pk_mul_f32 v[4:5], v[114:115], v[114:115] op_sel_hi:[0,1]\n"
    "v_pk_fma_f16 v6, v8, v9, v113 op_sel:[1,1,0] op_sel_hi:[1,0,1] neg_lo:[0,1,0]\n"
    "v_pk_fma_f32 v[0:1], v[114:115], v[114:115], v[4:5] op_sel:[1,1,0] op_sel_hi:[1,0,1] neg_lo:[0,1,0]\n"
    "ds_write_addtid_b32 v6 offset:8432\n"
    "v_cvt_pk_f16_f32 v3, v0, v1\n"
    "v_cvt_pk_f16_f32 v34, v10, v11\n"
    "ds_read_b128 v[64:67], v31 offset:0\n"
    "v_pk_mul_f32 v[14:15], v[0:1], v[0:1] op_sel_hi:[0,1]\n"
    "v_pk_mul_f16 v12, v30, v3 op_sel_hi:[0,1]\n"
    "ds_read_b128 v[68:71], v31 offset:32\n"
    "v_pk_mul_f16 v35, v30, v34 op_sel_hi:[0,1]\n"
    "v_pk_fma_f32 v[80:81], v[0:1], v[0:1], v[14:15] op_sel:[1,1,0] op_sel_hi:[1,0,1] neg_lo:[0,1,0]\n"
    "ds_read_b128 v[72:75], v31 offset:64\n"
    "v_pk_fma_f16 v16, v30, v3, v12 op_sel:[1,1,0] op_sel_hi:[1,0,1] neg_lo:[0,1,0]\n"
    "v_pk_fma_f16 v18, v30, v34, v35 op_sel:[1,1,0] op_sel_hi:[1,0,1] neg_lo:[0,1,0]\n"
    "ds_read_b128 v[4:7], v31 offset:96\n"
    "v_cvt_pk_f16_f32 v19, v80, v81\n"
    "v_pk_mul_f16 v8, v32, v34 op_sel_hi:[0,1]\n"
    "ds_read_b128 v[76:79], v31 offset:128\n"
    "v_pk_mul_f16 v9, v30, v19 op_sel_hi:[0,1]\n"
    "v_pk_fma_f16 v10, v32, v34, v8 op_sel:[1,1,0] op_sel_hi:[1,0,1] neg_lo:[0,1,0]\n"
    "ds_read_b128 v[84:87], v31 offset:160\n"
    "v_pk_fma_f16 v11, v30, v19, v9 op_sel:[1,1,0] op_sel_hi:[1,0,1] neg_lo:[0,1,0]\n"
    "v_cvt_pk_f16_f32 v0, v82, v83\n"
    "ds_read_b128 v[88:91], v31 offset:192\n"
    "s_setprio 1\n"
    "v_pk_mul_f16 v35, v16, v19 op_sel_hi:[0,1]\n"
    "v_pk_mul_f16 v1, v30, v0 op_sel_hi:[0,1]\n"
    "ds_read_b128 v[92:95], v31 offset:224\n"
    "v_pk_fma_f16 v3, v16, v19, v35 op_sel:[1,1,0] op_sel_hi:[1,0,1] neg_lo:[0,1,0]\n"
    "v_pk_fma_f16 v12, v30, v0, v1 op_sel:[1,1,0] op_sel_hi:[1,0,1] neg_lo:[0,1,0]\n"
    "v_pk_mul_f16 v14, v11, v19 op_sel_hi:[0,1]\n"
    "v_pk_mul_f16 v15, v32, v0 op_sel_hi:[0,1]\n"
    "ds_write_addtid_b32 v30 offset:272\n"
    "ds_write_addtid_b32 v32 offset:544\n"
    "v_pk_fma_f16 v8, v11, v19, v14 op_sel:[1,1,0] op_sel_hi:[1,0,1] neg_lo:[0,1,0]\n"
    "v_pk_fma_f16 v9, v32, v0, v15 op_sel:[1,1,0] op_sel_hi:[1,0,1] neg_lo:[0,1,0]\n"
    "global_load_dwordx4 v[80:83], v13, s[30:31] offset:0\n"
    "v_pk_mul_f16 v96, v3, v19 op_sel_hi:[0,1]\n"
    "ds_write_addtid_b32 v16 offset:1360\n"
    "ds_write_addtid_b32 v18 offset:816\n"
    "v_pk_mul_f16 v35, v18, v0 op_sel_hi:[0,1]\n"
    "v_pk_fma_f16 v1, v3, v19, v96 op_sel:[1,1,0] op_sel_hi:[1,0,1] neg_lo:[0,1,0]\n"
    "v_pk_fma_f16 v97, v18, v0, v35 op_sel:[1,1,0] op_sel_hi:[1,0,1] neg_lo:[0,1,0]\n"
    "v_pk_mul_f16 v98, v8, v19 op_sel_hi:[0,1]\n"
    "ds_write_addtid_b32 v11 offset:5712\n"
    "ds_write_addtid_b32 v10 offset:1088\n"
    "v_pk_mul_f16 v30, v10, v0 op_sel_hi:[0,1]\n"
    "v_pk_fma_f16 v14, v8, v19, v98 op_sel:[1,1,0] op_sel_hi:[1,0,1] neg_lo:[0,1,0]\n"
    "v_pk_fma_f16 v15, v10, v0, v30 op_sel:[1,1,0] op_sel_hi:[1,0,1] neg_lo:[0,1,0]\n"
    "s_waitcnt lgkmcnt(0)\n"
    "s_lshr_b32 s37, s37, s49\n"
    "s_and_b32 s37, s37, 0xffff\n"
    "ds_write_addtid_b32 v3 offset:6256\n"
    "ds_write_addtid_b32 v12 offset:2448\n"
    "v_pk_mul_f16 v32, v1, v19 op_sel_hi:[0,1]\n"
    "v_pk_mul_f16 v96, v16, v34 op_sel_hi:[0,1]\n"
    "v_pk_fma_f16 v18, v1, v19, v32 op_sel:[1,1,0] op_sel_hi:[1,0,1] neg_lo:[0,1,0]\n"
    "v_pk_fma_f16 v35, v16, v34, v96 op_sel:[1,1,0] op_sel_hi:[1,0,1] neg_lo:[0,1,0]\n"
    "ds_write_addtid_b32 v8 offset:4624\n"
    "ds_write_addtid_b32 v9 offset:2720\n"
    "v_pk_mul_f16 v99, v14, v19 op_sel_hi:[0,1]\n"
    "v_pk_mul_f16 v98, v16, v0 op_sel_hi:[0,1]\n"
    "v_pk_fma_f16 v30, v14, v19, v99 op_sel:[1,1,0] op_sel_hi:[1,0,1] neg_lo:[0,1,0]\n"
    "v_mul_u32_u24 v60, s37, v60\n"
    "ds_write_addtid_b32 v1 offset:5168\n"
    "ds_write_addtid_b32 v97 offset:2992\n"
    "v_mul_u32_u24 v61, s37, v61\n"
    "v_mul_u32_u24 v62, s37, v62\n"
    "v_mul_u32_u24 v63, s37, v63\n"
    "v_pk_fma_f16 v12, v16, v0, v98 op_sel:[1,1,0] op_sel_hi:[1,0,1] neg_lo:[0,1,0]\n"
    "ds_write_addtid_b32 v14 offset:4896\n"
    "ds_write_addtid_b32 v15 offset:3264\n"
    "v_pk_mul_f16 v32, v35, v0 op_sel_hi:[0,1]\n"
    "v_pk_mul_f16 v34, v11, v0 op_sel_hi:[0,1]\n"
    "v_pk_fma_f16 v19, v35, v0, v32 op_sel:[1,1,0] op_sel_hi:[1,0,1] neg_lo:[0,1,0]\n"
    "v_pk_fma_f16 v8, v11, v0, v34 op_sel:[1,1,0] op_sel_hi:[1,0,1] neg_lo:[0,1,0]\n"
    "ds_write_addtid_b32 v18 offset:5440\n"
    "ds_write_addtid_b32 v35 offset:1632\n"
    "v_pk_mul_f16 v1, v3, v0 op_sel_hi:[0,1]\n"
    "ds_write_addtid_b32 v30 offset:3536\n"
    "ds_write_addtid_b32 v12 offset:1904\n"
    "v_pk_fma_f16 v16, v3, v0, v1 op_sel:[1,1,0] op_sel_hi:[1,0,1] neg_lo:[0,1,0]\n"
    "ds_write_addtid_b32 v19 offset:2176\n"
    "ds_write_addtid_b32 v8 offset:5984\n"
    "ds_write_addtid_b32 v16 offset:6528\n"
    "ds_read_b128 v[96:99], v31 offset:272\n"
    "ds_read_b128 v[100:103], v31 offset:304\n"
    "ds_read_b128 v[104:107], v31 offset:336\n"
    "ds_read_b128 v[108:111], v31 offset:368\n"
    "ds_read_b128 v[112:115], v31 offset:400\n"
    "ds_read_b128 v[116:119], v31 offset:432\n"
    "ds_read_b128 v[120:123], v31 offset:464\n"
    "ds_read_b128 v[124:127], v31 offset:496\n"
    "ds_read_b32 v30, v2\n"
    "global_load_dwordx4 v[128:131], v13, s[30:31] offset:1280\n"
    "global_load_dwordx4 v[8:11], v13, s[30:31] offset:2560\n"
    "global_load_dwordx4 v[132:135], v13, s[30:31] offset:3840\n"
    "s_waitcnt lgkmcnt(8)\n"
    "v_mfma_f32_32x32x16_f16 v[168:183], v[96:99], v[64:67], v[168:183]\n"
    "s_waitcnt lgkmcnt(0)\n"
    "v_cmp_ne_u32 vcc, 0, v30\n"
    "s_cmp_eq_u64 vcc, -1\n"
    "s_cbranch_scc1 .Lok1B%=\n"
    ".Lsp1B%=:\n"
    "s_sleep 1\n"
    "ds_read_b32 v30, v2\n"
    "s_waitcnt lgkmcnt(0)\n"
    "v_cmp_ne_u32 vcc, 0, v30\n"
    "s_cmp_eq_u64 vcc, -1\n"
    "s_cbranch_scc0 .Lsp1B%=\n"
    ".Lok1B%=:\n"
    "s_setprio 0\n"
    "ds_read_b128 v[136:139], v22 offset:512\n"
    "ds_read_b128 v[140:143], v22 offset:496\n"
    "v_mfma_f32_32x32x16_f16 v[168:183], v[100:103], v[68:71], v[168:183]\n"
    "ds_read_b128 v[144:147], v22 offset:1024\n"
    "ds_read_b128 v[148:151], v22 offset:1008\n"
    "v_mfma_f32_32x32x16_f16 v[168:183], v[104:107], v[72:75], v[168:183]\n"
    "v_mfma_f32_32x32x16_f16 v[168:183], v[108:111], v[4:7], v[168:183]\n"
    "v_mfma_f32_32x32x16_f16 v[168:183], v[112:115], v[76:79], v[168:183]\n"
    "v_mfma_f32_32x32x16_f16 v[168:183], v[116:119], v[84:87], v[168:183]\n"
    "v_mfma_f32_32x32x16_f16 v[168:183], v[120:123], v[88:91], v[168:183]\n"
    "v_mfma_f32_32x32x16_f16 v[168:183], v[124:127], v[92:95], v[168:183]\n"
    "ds_read_b128 v[0:3], v22 offset:64\n"
    "ds_read_b128 v[12:15], v22 offset:128\n"
    "s_waitcnt lgkmcnt(5)\n"
    "v_mfma_f32_16x16x32_bf16 v[64:67], v[136:139], v[36:39], 0\n"
    "ds_read_b128 v[68:71], v22 offset:192\n"
    "ds_read_b128 v[96:99], v22 offset:256\n"
    "v_mfma_f32_16x16x32_bf16 v[100:103], v[136:139], v[40:43], 0\n"
    "ds_read_b128 v[72:75], v22 offset:320\n"
    "v_mfma_f32_16x16x32_bf16 v[4:7], v[136:139], v[44:47], 0\n"
    "s_waitcnt lgkmcnt(7)\n"
    "v_mfma_f32_16x16x32_bf16 v[76:79], v[140:143], v[40:43], 0\n"
    "v_mfma_f32_16x16x32_bf16 v[84:87], v[140:143], v[48:51], 0\n"
    "v_mfma_f32_16x16x32_bf16 v[88:91], v[136:139], v[52:55], 0\n"
    "v_cvt_pk_bf16_f32 v92, v64, v65\n"
    "v_mul_f32 v30, v33, v176\n"
    "s_waitcnt lgkmcnt(6)\n"
    "v_mfma_f32_16x16x32_bf16 v[104:107], v[144:147], v[36:39], 0\n"
    "v_cvt_pk_bf16_f32 v93, v66, v67\n"
    "v_readlane_b32 s53, v30, 32\n"
    "v_mfma_f32_16x16x32_bf16 v[108:111], v[144:147], v[40:43], 0\n"
    "v_mul_f32 v31, v33, v168\n"
    "v_cvt_pk_bf16_f32 v94, v100, v101\n"
    "v_mfma_f32_16x16x32_bf16 v[112:115], v[144:147], v[44:47], 0\n"
    "v_cvt_pk_bf16_f32 v95, v102, v103\n"
    "v_mov_b32 v16, s53\n"
    "s_waitcnt lgkmcnt(5)\n"
    "v_mfma_f32_16x16x32_bf16 v[116:119], v[148:151], v[40:43], 0\n"
    "v_mul_f32 v18, v33, v171\n"
    "v_readlane_b32 s56, v31, 0\n"
    "v_mfma_f32_16x16x32_bf16 v[120:123], v[148:151], v[48:51], 0\n"
    "v_mov_b32 v19, s53\n"
    "v_cvt_pk_bf16_f32 v64, v4, v5\n"
    "v_mfma_f32_16x16x32_bf16 v[124:127], v[144:147], v[52:55], 0\n"
    "v_cvt_pk_bf16_f32 v65, v6, v7\n"
    "v_mul_f32 v32, v33, v172\n"
    "v_mul_f32 v34, v33, v174\n"
    "v_mul_f32 v35, v33, v175\n"
    "v_readlane_b32 s57, v18, 63\n"
    "v_mov_b32_dpp v16, v31 wave_shl:1 row_mask:0xf bank_mask:0xf\n"
    "v_writelane_b32 v19, s56, 0\n"
    "v_readlane_b32 s55, v32, 32\n"
    "v_cndmask_b32_e64 v100, v34, v18, s[44:45]\n"
    "v_cndmask_b32_e64 v101, v35, v16, s[44:45]\n"
    "v_cvt_pk_bf16_f32 v66, v76, v77\n"
    "v_cvt_pk_bf16_f32 v67, v78, v79\n"
    "v_mul_f32 v102, v33, v169\n"
    "v_mul_f32 v103, v33, v170\n"
    "v_mul_f32 v4, v33, v173\n"
    "v_writelane_b32 v19, s57, 1\n"
    "v_cndmask_b32_e64 v5, v102, v31, s[44:45]\n"
    "v_cndmask_b32_e64 v6, v103, v102, s[44:45]\n"
    "v_cndmask_b32_e64 v7, v18, v103, s[44:45]\n"
    "v_cndmask_b32_e64 v136, v32, v103, s[44:45]\n"
    "v_cndmask_b32_e64 v34, v4, v103, s[44:45]\n"
    "v_mov_b32_dpp v35, v101 wave_shr:1 row_mask:0xf bank_mask:0xf bound_ctrl:0\n"
    "v_mov_b32_dpp v137, v100 wave_shr:1 row_mask:0xf bank_mask:0xf bound_ctrl:0\n"
    "v_writelane_b32 v19, s55, 2\n"
    "v_cvt_pk_bf16_f32 v76, v84, v85\n"
    "v_cvt_pk_bf16_f32 v77, v86, v87\n"
    "v_cvt_pk_bf16_f32 v140, v101, v100\n"
    "v_cvt_pk_bf16_f32 v141, v34, v136\n"
    "v_cvt_pk_bf16_f32 v142, v7, v6\n"
    "v_cvt_pk_bf16_f32 v143, v5, v31\n"
    "v_cvt_pk_bf16_f32 v144, v16, v101\n"
    "v_cvt_pk_bf16_f32 v145, v100, v34\n"
    "v_cvt_pk_bf16_f32 v146, v136, v7\n"
    "v_cvt_pk_bf16_f32 v147, v6, v5\n"
    "v_cvt_pk_bf16_f32 v148, v34, v136\n"
    "v_cvt_pk_bf16_f32 v149, v7, v6\n"
    "v_cvt_pk_bf16_f32 v150, v5, v31\n"
    "v_cvt_pk_bf16_f32 v151, v35, v137\n"
    "v_cvt_pk_bf16_f32 v84, v100, v34\n"
    "v_cvt_pk_bf16_f32 v85, v136, v7\n"
    "v_cvt_pk_bf16_f32 v86, v6, v5\n"
    "v_cvt_pk_bf16_f32 v87, v31, v35\n"
    "v_cvt_pk_bf16_f32 v19, v19, v19\n"
    "v_cvt_pk_bf16_f32 v78, v88, v89\n"
    "v_cvt_pk_bf16_f32 v79, v90, v91\n"
    "ds_write_b128 v23, v[140:143] offset:0\n"
    "ds_write_b128 v23, v[144:147] offset:1216\n"
    "ds_write_b128 v23, v[148:151] offset:2432\n"
    "ds_write_b128 v23, v[84:87] offset:3648\n"
    "s_mov_b64 exec, 0xfffff\n"
    "ds_write_b16 v28, v19\n"
    "s_mov_b64 exec, -1\n"
    "v_cvt_pk_bf16_f32 v100, v104, v105\n"
    "v_cvt_pk_bf16_f32 v101, v106, v107\n"
    "ds_read_b64 v[4:5], v25 offset:576\n"
    "ds_read_b64 v[6:7], v25 offset:584\n"
    "v_cvt_pk_bf16_f32 v102, v108, v109\n"
    "v_cvt_pk_bf16_f32 v103, v110, v111\n"
    "ds_read_b64 v[88:89], v25 offset:64\n"
    "ds_read_b64 v[90:91], v25 offset:72\n"
    "v_cvt_pk_bf16_f32 v136, v112, v113\n"
    "v_cvt_pk_bf16_f32 v137, v114, v115\n"
    "v_cvt_pk_bf16_f32 v138, v116, v117\n"
    "v_cvt_pk_bf16_f32 v139, v118, v119\n"
    "ds_read_b64 v[84:85], v25 offset:640\n"
    "ds_read_b64 v[86:87], v25 offset:648\n"
    "v_cvt_pk_bf16_f32 v104, v120, v121\n"
    "v_cvt_pk_bf16_f32 v105, v122, v123\n"
    "ds_read_b64 v[140:141], v25 offset:128\n"
    "ds_read_b64 v[142:143], v25 offset:136\n"
    "v_cvt_pk_bf16_f32 v106, v124, v125\n"
    "v_cvt_pk_bf16_f32 v107, v126, v127\n"
    "ds_read_b64 v[108:109], v25 offset:704\n"
    "ds_read_b64 v[110:111], v25 offset:712\n"
    "v_mul_f32 v23, v33, v177\n"
    "v_mul_f32 v28, v33, v178\n"
    "ds_read_b64 v[112:113], v25 offset:192\n"
    "ds_read_b64 v[114:115], v25 offset:200\n"
    "v_mul_f32 v31, v33, v179\n"
    "v_cndmask_b32_e64 v16, v30, 0, s[46:47]\n"
    "ds_read_b64 v[116:117], v25 offset:768\n"
    "ds_read_b64 v[118:119], v25 offset:776\n"
    "v_cvt_pk_bf16_f32 v18, v16, v23\n"
    "v_cvt_pk_bf16_f32 v19, v28, v31\n"
    "ds_read_b64 v[120:121], v25 offset:256\n"
    "s_waitcnt lgkmcnt(13)\n"
    "v_mfma_f32_16x16x32_bf16 v[124:127], v[4:7], v[0:3], 0\n"
    "ds_read_b64 v[122:123], v25 offset:264\n"
    "v_add_u32 v16, 640, v27\n"
    "ds_read_b64 v[144:145], v25 offset:832\n"
    "s_waitcnt lgkmcnt(13)\n"
    "v_mfma_f32_16x16x32_bf16 v[148:151], v[88:91], v[0:3], 0\n"
    "ds_read_b64 v[146:147], v25 offset:840\n"
    "ds_read_b128 v[32:35], v22 offset:384\n"
    "ds_read_b64 v[152:153], v25 offset:320\n"
    "ds_read_b64 v[154:155], v25 offset:328\n"
    "v_readlane_b32 s54, v30, 0\n"
    "s_waitcnt lgkmcnt(15)\n"
    "v_mfma_f32_16x16x32_bf16 v[124:127], v[84:87], v[12:15], v[124:127]\n"
    "ds_read_b64 v[156:157], v25 offset:896\n"
    "ds_read_b64 v[158:159], v25 offset:904\n"
    "v_cvt_pk_bf16_f32 v23, s54, 0\n"
    "s_waitcnt lgkmcnt(15)\n"
    "v_mfma_f32_16x16x32_bf16 v[148:151], v[140:143], v[12:15], v[148:151]\n"
    "ds_read_b64 v[160:161], v25 offset:384\n"
    "ds_read_b64 v[162:163], v25 offset:392\n"
    "ds_read_b128 v[0:3], v22 offset:448\n"
    "s_waitcnt lgkmcnt(15)\n"
    "v_mfma_f32_16x16x32_bf16 v[124:127], v[108:111], v[68:71], v[124:127]\n"
    "ds_read_b64 v[164:165], v25 offset:960\n"
    "ds_read_b64 v[166:167], v25 offset:968\n"
    "v_mul_u32_u24 v56, v23, v56\n"
    "s_waitcnt lgkmcnt(15)\n"
    "v_mfma_f32_16x16x32_bf16 v[148:151], v[112:115], v[68:71], v[148:151]\n"
    "ds_read_b64 v[168:169], v25 offset:448\n"
    "ds_read_b64 v[170:171], v25 offset:456\n"
    "ds_read_b128 v[172:175], v22 offset:512\n"
    "s_waitcnt lgkmcnt(15)\n"
    "v_mfma_f32_16x16x32_bf16 v[124:127], v[116:119], v[96:99], v[124:127]\n"
    "ds_read_b64 v[12:13], v25 offset:1024\n"
    "ds_read_b64 v[14:15], v25 offset:1032\n"
    "v_mul_u32_u24 v57, v23, v57\n"
    "v_mul_u32_u24 v58, v23, v58\n"
    "v_mfma_f32_16x16x32_bf16 v[148:151], v[120:123], v[96:99], v[148:151]\n"
    "ds_read_b64 v[176:177], v25 offset:512\n"
    "ds_read_b64 v[178:179], v25 offset:520\n"
    "ds_read_b128 v[68:71], v22 offset:576\n"
    "s_waitcnt lgkmcnt(15)\n"
    "v_mfma_f32_16x16x32_bf16 v[124:127], v[144:147], v[72:75], v[124:127]\n"
    "ds_read_b64 v[180:181], v25 offset:0\n"
    "ds_read_b64 v[182:183], v25 offset:8\n"
    "v_mfma_f32_16x16x32_bf16 v[148:151], v[152:155], v[72:75], v[148:151]\n"
    "ds_read_b128 v[184:187], v22 offset:640\n"
    "ds_write_b16 v17, v18 offset:32\n"
    "ds_write_b16_d16_hi v26, v18 offset:32\n"
    "s_waitcnt lgkmcnt(15)\n"
    "v_mfma_f32_16x16x32_bf16 v[124:127], v[156:159], v[32:35], v[124:127]\n"
    "ds_write_b16 v17, v18 offset:222\n"
    "ds_write_b16_d16_hi v26, v18 offset:222\n"
    "ds_write_b16 v17, v19 offset:0\n"
    "v_mfma_f32_16x16x32_bf16 v[148:151], v[160:163], v[32:35], v[148:151]\n"
    "ds_read_b128 v[96:99], v22 offset:704\n"
    "ds_write_b16_d16_hi v26, v19 offset:0\n"
    "ds_write_b16 v17, v19 offset:190\n"
    "s_waitcnt lgkmcnt(15)\n"
    "v_mfma_f32_16x16x32_bf16 v[124:127], v[164:167], v[0:3], v[124:127]\n"
    "ds_write_b16_d16_hi v26, v19 offset:190\n"
    "v_mul_u32_u24 v59, v23, v59\n"
    "v_add_u32 v23, 1280, v27\n"
    "v_mfma_f32_16x16x32_bf16 v[148:151], v[168:171], v[0:3], v[148:151]\n"
    "ds_read_b128 v[72:75], v22 offset:768\n"
    "v_mfma_f32_16x16x32_bf16 v[188:191], v[60:63], v[172:175], 0\n"
    "s_waitcnt lgkmcnt(15)\n"
    "v_mfma_f32_16x16x32_bf16 v[124:127], v[12:15], v[172:175], v[124:127]\n"
    "s_waitcnt lgkmcnt(14)\n"
    "v_mfma_f32_16x16x32_bf16 v[148:151], v[176:179], v[172:175], v[148:151]\n"
    "s_waitcnt lgkmcnt(13)\n"
    "v_mfma_f32_16x16x32_bf16 v[148:151], v[4:7], v[68:71], v[148:151]\n"
    "s_waitcnt lgkmcnt(11)\n"
    "v_mfma_f32_16x16x32_bf16 v[32:35], v[180:183], v[172:175], 0\n"
    "ds_read_b128 v[192:195], v22 offset:832\n"
    "ds_read2_b32 v[196:197], v27 offset0:16 offset1:17\n"
    "v_mfma_f32_16x16x32_bf16 v[32:35], v[88:91], v[68:71], v[32:35]\n"
    "ds_read_b128 v[200:203], v22 offset:896\n"
    "v_cvt_pk_bf16_f32 v188, 0, v188\n"
    "s_waitcnt lgkmcnt(13)\n"
    "v_mfma_f32_16x16x32_bf16 v[32:35], v[140:143], v[184:187], v[32:35]\n"
    "ds_read_b128 v[0:3], v22 offset:960\n"
    "v_cvt_pk_bf16_f32 v204, 0, v124\n"
    "v_mfma_f32_16x16x32_bf16 v[148:151], v[84:87], v[184:187], v[148:151]\n"
    "ds_read2_b32 v[198:199], v27 offset0:96 offset1:97\n"
    "v_cvt_pk_bf16_f32 v205, 0, v125\n"
    "s_waitcnt lgkmcnt(9)\n"
    "v_mfma_f32_16x16x32_bf16 v[32:35], v[112:115], v[96:99], v[32:35]\n"
    "ds_read_b128 v[172:175], v22 offset:1024\n"
    "v_cvt_pk_bf16_f32 v206, 0, v126\n"
    "v_mfma_f32_16x16x32_bf16 v[148:151], v[108:111], v[96:99], v[148:151]\n"
    "ds_read2_b32 v[68:69], v16 offset0:16 offset1:17\n"
    "v_cvt_pk_bf16_f32 v207, 0, v127\n"
    "s_waitcnt lgkmcnt(7)\n"
    "v_mfma_f32_16x16x32_bf16 v[148:151], v[116:119], v[72:75], v[148:151]\n"
    "ds_read2_b32 v[70:71], v16 offset0:96 offset1:97\n"
    "v_cvt_pk_bf16_f32 v189, 0, v189\n"
    "v_mfma_f32_16x16x32_bf16 v[32:35], v[120:123], v[72:75], v[32:35]\n"
    "ds_read_b128 v[184:187], v22 offset:1088\n"
    "v_cvt_pk_bf16_f32 v190, 0, v190\n"
    "v_cvt_pk_bf16_f32 v191, 0, v191\n"
    "v_add_f32 v204, v204, v188\n"
    "v_add_f32 v205, v205, v189\n"
    "v_add_f32 v206, v206, v190\n"
    "v_add_f32 v207, v207, v191\n"
    "v_cvt_pk_bf16_f32 v18, v204, v205\n"
    "v_cvt_pk_bf16_f32 v19, v206, v207\n"
    "s_waitcnt lgkmcnt(8)\n"
    "v_mfma_f32_16x16x32_bf16 v[148:151], v[144:147], v[192:195], v[148:151]\n"
    "ds_write_b64 v24, v[18:19] offset:0\n"
    "v_mfma_f32_16x16x32_bf16 v[32:35], v[152:155], v[192:195], v[32:35]\n"
    "ds_read_b128 v[72:75], v22 offset:1152\n"
    "s_waitcnt lgkmcnt(8)\n"
    "v_mfma_f32_16x16x32_bf16 v[148:151], v[156:159], v[200:203], v[148:151]\n"
    "v_mfma_f32_16x16x32_bf16 v[32:35], v[160:163], v[200:203], v[32:35]\n"
    "ds_read_b128 v[96:99], v22 offset:1216\n"
    "s_waitcnt lgkmcnt(8)\n"
    "v_mfma_f32_16x16x32_bf16 v[148:151], v[164:167], v[0:3], v[148:151]\n"
    "v_mfma_f32_16x16x32_bf16 v[32:35], v[168:171], v[0:3], v[32:35]\n"
    "ds_read_b128 v[124:127], v22 offset:1280\n"
    "s_waitcnt lgkmcnt(7)\n"
    "v_mfma_f32_16x16x32_bf16 v[188:191], v[60:63], v[172:175], 0\n"
    "v_mfma_f32_16x16x32_bf16 v[148:151], v[12:15], v[172:175], v[148:151]\n"
    "v_mfma_f32_16x16x32_bf16 v[32:35], v[176:179], v[172:175], v[32:35]\n"
    "v_mfma_f32_16x16x32_bf16 v[204:207], v[180:183], v[172:175], 0\n"
    "ds_read_b128 v[192:195], v22 offset:1344\n"
    "ds_read2_b32 v[208:209], v27 offset0:8 offset1:9\n"
    "s_waitcnt lgkmcnt(6)\n"
    "v_mfma_f32_16x16x32_bf16 v[32:35], v[4:7], v[184:187], v[32:35]\n"
    "ds_read2_b32 v[210:211], v27 offset0:88 offset1:89\n"
    "ds_read2_b32 v[200:201], v16 offset0:8 offset1:9\n"
    "v_mfma_f32_16x16x32_bf16 v[204:207], v[88:91], v[184:187], v[204:207]\n"
    "ds_read_b128 v[0:3], v22 offset:1408\n"
    "v_cvt_pk_bf16_f32 v212, 0, v148\n"
    "s_waitcnt lgkmcnt(7)\n"
    "v_mfma_f32_16x16x32_bf16 v[32:35], v[84:87], v[72:75], v[32:35]\n"
    "v_cvt_pk_bf16_f32 v213, 0, v149\n"
    "v_cvt_pk_bf16_f32 v214, 0, v150\n"
    "v_mfma_f32_16x16x32_bf16 v[204:207], v[140:143], v[72:75], v[204:207]\n"
    "ds_read_b128 v[172:175], v22 offset:1472\n"
    "v_cvt_pk_bf16_f32 v215, 0, v151\n"
    "s_waitcnt lgkmcnt(7)\n"
    "v_mfma_f32_16x16x32_bf16 v[32:35], v[108:111], v[96:99], v[32:35]\n"
    "v_cvt_pk_bf16_f32 v188, 0, v188\n"
    "v_cvt_pk_bf16_f32 v189, 0, v189\n"
    "v_mfma_f32_16x16x32_bf16 v[204:207], v[112:115], v[96:99], v[204:207]\n"
    "ds_read_b128 v[184:187], v22 offset:1536\n"
    "v_cvt_pk_bf16_f32 v190, 0, v190\n"
    "s_waitcnt lgkmcnt(7)\n"
    "v_mfma_f32_16x16x32_bf16 v[32:35], v[116:119], v[124:127], v[32:35]\n"
    "v_cvt_pk_bf16_f32 v191, 0, v191\n"
    "v_add_f32 v212, v212, v188\n"
    "v_mfma_f32_16x16x32_bf16 v[204:207], v[120:123], v[124:127], v[204:207]\n"
    "ds_read_b128 v[72:75], v22 offset:1600\n"
    "v_add_f32 v213, v213, v189\n"
    "v_mfma_f32_16x16x32_bf16 v[32:35], v[92:95], v[196:199], v[32:35]\n"
    "v_add_f32 v214, v214, v190\n"
    "v_add_f32 v215, v215, v191\n"
    "v_mfma_f32_16x16x32_bf16 v[32:35], v[64:67], v[68:71], v[32:35]\n"
    "v_cvt_pk_bf16_f32 v18, v212, v213\n"
    "v_cvt_pk_bf16_f32 v19, v214, v215\n"
    "s_waitcnt lgkmcnt(7)\n"
    "v_mfma_f32_16x16x32_bf16 v[32:35], v[144:147], v[192:195], v[32:35]\n"
    "ds_write_b64 v24, v[18:19] offset:512\n"
    "ds_read2_b32 v[202:203], v16 offset0:88 offset1:89\n"
    "v_mfma_f32_16x16x32_bf16 v[204:207], v[152:155], v[192:195], v[204:207]\n"
    "ds_read_b128 v[96:99], v22 offset:1664\n"
    "ds_read_b128 v[124:127], v22 offset:16\n"
    "s_waitcnt lgkmcnt(7)\n"
    "v_mfma_f32_16x16x32_bf16 v[32:35], v[156:159], v[0:3], v[32:35]\n"
    "s_waitcnt vmcnt(2)\n"
    "v_perm_b32 v30, v128, v80, s41\n"
    "s_waitcnt vmcnt(0)\n"
    "v_perm_b32 v31, v132, v8, s41\n"
    "v_mfma_f32_16x16x32_bf16 v[204:207], v[160:163], v[0:3], v[204:207]\n"
    "ds_read_b128 v[148:151], v22 offset:1536\n"
    "ds_read_b128 v[188:191], v22 offset:1728\n"
    "s_waitcnt lgkmcnt(8)\n"
    "v_mfma_f32_16x16x32_bf16 v[32:35], v[164:167], v[172:175], v[32:35]\n"
    "s_waitcnt lgkmcnt(5)\n"
    "s_barrier\n"
    "s_setprio 1\n"
    "ds_read_b64_tr_b16 v[192:193], v21 offset:512\n"
    "v_mfma_f32_16x16x32_bf16 v[204:207], v[168:171], v[172:175], v[204:207]\n"
    "ds_read_b64_tr_b16 v[194:195], v21 offset:35584\n"
    "ds_read_b128 v[212:215], v22 offset:1792\n"
    "v_mfma_f32_16x16x32_bf16 v[216:219], v[60:63], v[184:187], 0\n"
    "ds_write_b64 v20, v[30:31] offset:0\n"
    "v_perm_b32 v18, v128, v80, s42\n"
    "v_mfma_f32_16x16x32_bf16 v[32:35], v[12:15], v[184:187], v[32:35]\n"
    "ds_read_b128 v[0:3], v22 offset:1520\n"
    "v_perm_b32 v19, v132, v8, s42\n"
    "v_mfma_f32_16x16x32_bf16 v[204:207], v[176:179], v[184:187], v[204:207]\n"
    "ds_write_b64 v20, v[18:19] offset:4640\n"
    "v_perm_b32 v220, v129, v81, s41\n"
    "v_mfma_f32_16x16x32_bf16 v[172:175], v[180:183], v[184:187], 0\n"
    "ds_read_b128 v[224:227], v22 offset:1856\n"
    "v_cvt_pk_bf16_f32 v216, 0, v216\n"
    "v_mfma_f32_16x16x32_bf16 v[204:207], v[4:7], v[72:75], v[204:207]\n"
    "v_cvt_pk_bf16_f32 v228, 0, v32\n"
    "v_cvt_pk_bf16_f32 v229, 0, v33\n"
    "v_mfma_f32_16x16x32_bf16 v[172:175], v[88:91], v[72:75], v[172:175]\n"
    "ds_read_b128 v[232:235], v22 offset:1920\n"
    "v_cvt_pk_bf16_f32 v230, 0, v34\n"
    "s_waitcnt lgkmcnt(11)\n"
    "v_mfma_f32_16x16x32_bf16 v[204:207], v[84:87], v[96:99], v[204:207]\n"
    "v_cvt_pk_bf16_f32 v231, 0, v35\n"
    "v_cvt_pk_bf16_f32 v217, 0, v217\n"
    "v_mfma_f32_16x16x32_bf16 v[172:175], v[140:143], v[96:99], v[172:175]\n"
    "s_waitcnt lgkmcnt(6)\n"
    "global_store_dwordx4 v29, v[192:195], s[34:35]\n"
    "s_add_u32 s34, s34, 1310720\n"
    "s_addc_u32 s35, s35, 0\n"
    "v_cvt_pk_bf16_f32 v218, 0, v218\n"
    "v_mfma_f32_16x16x32_bf16 v[184:187], v[148:151], v[36:39], 0\n"
    "ds_read_b128 v[236:239], v22 offset:1984\n"
    "v_cvt_pk_bf16_f32 v219, 0, v219\n"
    "v_mfma_f32_16x16x32_bf16 v[204:207], v[108:111], v[188:191], v[204:207]\n"
    "v_add_f32 v228, v228, v216\n"
    "v_add_f32 v229, v229, v217\n"
    "v_mfma_f32_16x16x32_bf16 v[172:175], v[112:115], v[188:191], v[172:175]\n"
    "ds_read_b128 v[32:35], v22 offset:2048\n"
    "v_add_f32 v230, v230, v218\n"
    "v_mfma_f32_16x16x32_bf16 v[72:75], v[148:151], v[40:43], 0\n"
    "v_add_f32 v231, v231, v219\n"
    "v_cvt_pk_bf16_f32 v222, v228, v229\n"
    "s_setprio 0\n"
    "s_waitcnt lgkmcnt(7)\n"
    "v_mfma_f32_16x16x32_bf16 v[204:207], v[116:119], v[212:215], v[204:207]\n"
    "v_cvt_pk_bf16_f32 v223, v230, v231\n"
    "v_perm_b32 v221, v133, v9, s41\n"
    "v_mfma_f32_16x16x32_bf16 v[172:175], v[120:123], v[212:215], v[172:175]\n"
    "ds_write_b64 v24, v[222:223] offset:1024\n"
    "v_perm_b32 v18, v129, v81, s42\n"
    "v_mfma_f32_16x16x32_bf16 v[96:99], v[148:151], v[44:47], 0\n"
    "ds_write_b64 v20, v[220:221] offset:9280\n"
    "v_perm_b32 v19, v133, v9, s42\n"
    "v_mfma_f32_16x16x32_bf16 v[204:207], v[100:103], v[196:199], v[204:207]\n"
    "ds_write_b64 v20, v[18:19] offset:13920\n"
    "v_perm_b32 v30, v130, v82, s41\n"
    "v_mfma_f32_16x16x32_bf16 v[204:207], v[136:139], v[68:71], v[204:207]\n"
    "v_perm_b32 v31, v134, v10, s41\n"
    "v_perm_b32 v188, v130, v82, s42\n"
    "v_mfma_f32_16x16x32_bf16 v[204:207], v[92:95], v[208:211], v[204:207]\n"
    "ds_write_b64 v20, v[30:31] offset:18560\n"
    "v_perm_b32 v189, v134, v10, s42\n"
    "v_mfma_f32_16x16x32_bf16 v[204:207], v[64:67], v[200:203], v[204:207]\n"
    "ds_write_b64 v20, v[188:189] offset:23200\n"
    "v_perm_b32 v190, v131, v83, s41\n"
    "v_mfma_f32_16x16x32_bf16 v[204:207], v[56:59], v[124:127], v[204:207]\n"
    "v_perm_b32 v191, v135, v11, s41\n"
    "v_perm_b32 v18, v131, v83, s42\n"
    "s_waitcnt lgkmcnt(8)\n"
    "v_mfma_f32_16x16x32_bf16 v[204:207], v[144:147], v[224:227], v[204:207]\n"
    "ds_write_b64 v20, v[190:191] offset:27840\n"
    "v_perm_b32 v19, v135, v11, s42\n"
    "v_mfma_f32_16x16x32_bf16 v[172:175], v[152:155], v[224:227], v[172:175]\n"
    "ds_write_b64 v20, v[18:19] offset:32480\n"
    "v_cvt_pk_bf16_f32 v192, v184, v185\n"
    "v_mfma_f32_16x16x32_bf16 v[212:215], v[0:3], v[40:43], 0\n"
    "v_cvt_pk_bf16_f32 v193, v186, v187\n"
    "v_cvt_pk_bf16_f32 v194, v72, v73\n"
    "s_waitcnt lgkmcnt(9)\n"
    "v_mfma_f32_16x16x32_bf16 v[204:207], v[156:159], v[232:235], v[204:207]\n"
    "v_cvt_pk_bf16_f32 v195, v74, v75\n"
    "v_cvt_pk_bf16_f32 v80, v96, v97\n"
    "v_mfma_f32_16x16x32_bf16 v[172:175], v[160:163], v[232:235], v[172:175]\n"
    "ds_read_b128 v[124:127], v22 offset:2048\n"
    "v_cvt_pk_bf16_f32 v81, v98, v99\n"
    "v_mfma_f32_16x16x32_bf16 v[8:11], v[0:3], v[48:51], 0\n"
    "s_waitcnt lgkmcnt(1)\n"
    "s_barrier\n"
    "s_setprio 1\n"
    "ds_read_b128 v[128:131], v22 offset:2112\n"
    "v_mfma_f32_16x16x32_bf16 v[204:207], v[164:167], v[236:239], v[204:207]\n"
    "ds_read_b128 v[132:135], v22 offset:2176\n"
    "v_cvt_pk_bf16_f32 v82, v212, v213\n"
    "v_mfma_f32_16x16x32_bf16 v[172:175], v[168:171], v[236:239], v[172:175]\n"
    "ds_read_b128 v[72:75], v22 offset:2240\n"
    "v_cvt_pk_bf16_f32 v83, v214, v215\n"
    "v_mfma_f32_16x16x32_bf16 v[184:187], v[148:151], v[52:55], 0\n"
    "ds_read_b128 v[96:99], v22 offset:2304\n"
    "v_cvt_pk_bf16_f32 v0, v8, v9\n"
    "v_mfma_f32_16x16x32_bf16 v[188:191], v[60:63], v[32:35], 0\n"
    "ds_read2_b32 v[216:217], v27 offset0:0 offset1:1\n"
    "v_cvt_pk_bf16_f32 v1, v10, v11\n"
    "v_mfma_f32_16x16x32_bf16 v[204:207], v[12:15], v[32:35], v[204:207]\n"
    "ds_read2_b32 v[218:219], v27 offset0:80 offset1:81\n"
    "ds_read2_b32 v[220:221], v16 offset0:0 offset1:1\n"
    "v_mfma_f32_16x16x32_bf16 v[172:175], v[176:179], v[32:35], v[172:175]\n"
    "ds_read2_b32 v[222:223], v16 offset0:80 offset1:81\n"
    "v_cvt_pk_bf16_f32 v2, v184, v185\n"
    "v_mfma_f32_16x16x32_bf16 v[148:151], v[180:183], v[32:35], 0\n"
    "ds_read2_b32 v[212:213], v23 offset0:24 offset1:25\n"
    "v_cvt_pk_bf16_f32 v3, v186, v187\n"
    "v_cvt_pk_bf16_f32 v224, 0, v204\n"
    "v_cvt_pk_bf16_f32 v225, 0, v205\n"
    "v_cvt_pk_bf16_f32 v226, 0, v206\n"
    "v_cvt_pk_bf16_f32 v227, 0, v207\n"
    "s_waitcnt lgkmcnt(9)\n"
    "v_mfma_f32_16x16x32_bf16 v[8:11], v[124:127], v[36:39], 0\n"
    "ds_read2_b32 v[214:215], v23 offset0:112 offset1:113\n"
    "v_cvt_pk_bf16_f32 v188, 0, v188\n"
    "v_mfma_f32_16x16x32_bf16 v[16:19], v[124:127], v[40:43], 0\n"
    "ds_read_b128 v[228:231], v22 offset:528\n"
    "v_cvt_pk_bf16_f32 v189, 0, v189\n"
    "s_waitcnt lgkmcnt(10)\n"
    "v_mfma_f32_16x16x32_bf16 v[172:175], v[4:7], v[128:131], v[172:175]\n"
    "ds_read_b128 v[32:35], v22 offset:2368\n"
    "v_cvt_pk_bf16_f32 v190, 0, v190\n"
    "v_mfma_f32_16x16x32_bf16 v[148:151], v[88:91], v[128:131], v[148:151]\n"
    "ds_read_b128 v[184:187], v22 offset:2032\n"
    "v_cvt_pk_bf16_f32 v191, 0, v191\n"
    "s_waitcnt lgkmcnt(11)\n"
    "v_mfma_f32_16x16x32_bf16 v[172:175], v[84:87], v[132:135], v[172:175]\n"
    "ds_read_b128 v[204:207], v22 offset:2432\n"
    "v_add_f32 v224, v224, v188\n"
    "v_mfma_f32_16x16x32_bf16 v[148:151], v[140:143], v[132:135], v[148:151]\n"
    "ds_read_b128 v[232:235], v22 offset:2496\n"
    "v_add_f32 v225, v225, v189\n"
    "s_waitcnt lgkmcnt(12)\n"
    "v_mfma_f32_16x16x32_bf16 v[172:175], v[108:111], v[72:75], v[172:175]\n"
    "v_add_f32 v226, v226, v190\n"
    "v_add_f32 v227, v227, v191\n"
    "v_mfma_f32_16x16x32_bf16 v[148:151], v[112:115], v[72:75], v[148:151]\n"
    "ds_read_b128 v[128:131], v22 offset:2560\n"
    "v_cvt_pk_bf16_f32 v26, v224, v225\n"
    "s_waitcnt lgkmcnt(12)\n"
    "v_mfma_f32_16x16x32_bf16 v[172:175], v[116:119], v[96:99], v[172:175]\n"
    "v_cvt_pk_bf16_f32 v27, v226, v227\n"
    "v_cvt_pk_bf16_f32 v236, v8, v9\n"
    "v_mfma_f32_16x16x32_bf16 v[148:151], v[120:123], v[96:99], v[148:151]\n"
    "ds_write_b64 v24, v[26:27] offset:1536\n"
    "v_cvt_pk_bf16_f32 v237, v10, v11\n"
    "v_mfma_f32_16x16x32_bf16 v[132:135], v[124:127], v[44:47], 0\n"
    "ds_read_b128 v[240:243], v22 offset:2624\n"
    "v_cvt_pk_bf16_f32 v238, v16, v17\n"
    "v_mfma_f32_16x16x32_bf16 v[172:175], v[192:195], v[196:199], v[172:175]\n"
    "v_cvt_pk_bf16_f32 v239, v18, v19\n"
    "v_mfma_f32_16x16x32_bf16 v[172:175], v[80:83], v[68:71], v[172:175]\n"
    "ds_read2_b32 v[72:73], v23 offset0:16 offset1:17\n"
    "ds_read2_b32 v[74:75], v23 offset0:104 offset1:105\n"
    "v_mfma_f32_16x16x32_bf16 v[172:175], v[100:103], v[208:211], v[172:175]\n"
    "v_cvt_pk_bf16_f32 v96, v132, v133\n"
    "v_cvt_pk_bf16_f32 v97, v134, v135\n"
    "v_mfma_f32_16x16x32_bf16 v[172:175], v[136:139], v[200:203], v[172:175]\n"
    "ds_read_b128 v[8:11], v22 offset:1040\n"
    "s_waitcnt lgkmcnt(15)\n"
    "v_mfma_f32_16x16x32_bf16 v[172:175], v[92:95], v[216:219], v[172:175]\n"
    "s_setprio 0\n"
    "s_waitcnt lgkmcnt(13)\n"
    "v_mfma_f32_16x16x32_bf16 v[172:175], v[64:67], v[220:223], v[172:175]\n"
    "s_waitcnt lgkmcnt(11)\n"
    "v_mfma_f32_16x16x32_bf16 v[172:175], v[76:79], v[212:215], v[172:175]\n"
    "s_waitcnt lgkmcnt(10)\n"
    "v_mfma_f32_16x16x32_bf16 v[172:175], v[56:59], v[228:231], v[172:175]\n"
    "s_waitcnt lgkmcnt(9)\n"
    "v_mfma_f32_16x16x32_bf16 v[172:175], v[144:147], v[32:35], v[172:175]\n"
    "v_mfma_f32_16x16x32_bf16 v[148:151], v[152:155], v[32:35], v[148:151]\n"
    "ds_read_b128 v[16:19], v22 offset:2688\n"
    "s_waitcnt lgkmcnt(9)\n"
    "v_mfma_f32_16x16x32_bf16 v[188:191], v[184:187], v[40:43], 0\n"
    "s_waitcnt lgkmcnt(8)\n"
    "v_mfma_f32_16x16x32_bf16 v[172:175], v[156:159], v[204:207], v[172:175]\n"
    "v_mfma_f32_16x16x32_bf16 v[148:151], v[160:163], v[204:207], v[148:151]\n"
    "ds_read_b128 v[132:135], v22 offset:2560\n"
    "ds_read_b128 v[92:95], v22 offset:2752\n"
    "v_mfma_f32_16x16x32_bf16 v[224:227], v[184:187], v[48:51], 0\n"
    "ds_read_b128 v[64:67], v22 offset:2544\n"
    "s_waitcnt lgkmcnt(10)\n"
    "v_mfma_f32_16x16x32_bf16 v[172:175], v[164:167], v[232:235], v[172:175]\n"
    "v_mfma_f32_16x16x32_bf16 v[148:151], v[168:171], v[232:235], v[148:151]\n"
    "ds_read_b128 v[228:231], v22 offset:2816\n"
    "v_cvt_pk_bf16_f32 v98, v188, v189\n"
    "v_mfma_f32_16x16x32_bf16 v[32:35], v[124:127], v[52:55], 0\n"
    "v_cvt_pk_bf16_f32 v99, v190, v191\n"
    "s_waitcnt lgkmcnt(10)\n"
    "v_mfma_f32_16x16x32_bf16 v[244:247], v[60:63], v[128:131], 0\n"
    "v_cvt_pk_bf16_f32 v204, v224, v225\n"
    "v_cvt_pk_bf16_f32 v205, v226, v227\n"
    "v_mfma_f32_16x16x32_bf16 v[172:175], v[12:15], v[128:131], v[172:175]\n"
    "v_mfma_f32_16x16x32_bf16 v[148:151], v[176:179], v[128:131], v[148:151]\n"
    "v_mfma_f32_16x16x32_bf16 v[48:51], v[180:183], v[128:131], 0\n"
    "ds_read_b128 v[184:187], v22 offset:2880\n"
    "v_cvt_pk_bf16_f32 v206, v32, v33\n"
    "s_waitcnt lgkmcnt(9)\n"
    "v_mfma_f32_16x16x32_bf16 v[148:151], v[4:7], v[240:243], v[148:151]\n"
    "v_cvt_pk_bf16_f32 v207, v34, v35\n"
    "v_cvt_pk_bf16_f32 v244, 0, v244\n"
    "v_mfma_f32_16x16x32_bf16 v[48:51], v[88:91], v[240:243], v[48:51]\n"
    "ds_read_b128 v[52:55], v22 offset:2944\n"
    "v_cvt_pk_bf16_f32 v124, 0, v172\n"
    "s_waitcnt lgkmcnt(6)\n"
    "v_mfma_f32_16x16x32_bf16 v[148:151], v[84:87], v[16:19], v[148:151]\n"
    "v_cvt_pk_bf16_f32 v125, 0, v173\n"
    "v_cvt_pk_bf16_f32 v126, 0, v174\n"
    "v_mfma_f32_16x16x32_bf16 v[48:51], v[140:143], v[16:19], v[48:51]\n"
    "ds_read_b128 v[128:131], v22 offset:3008\n"
    "v_cvt_pk_bf16_f32 v127, 0, v175\n"
    "s_waitcnt lgkmcnt(6)\n"
    "v_mfma_f32_16x16x32_bf16 v[188:191], v[132:135], v[36:39], 0\n"
    "v_cvt_pk_bf16_f32 v245, 0, v245\n"
    "v_cvt_pk_bf16_f32 v246, 0, v246\n"
    "s_waitcnt lgkmcnt(5)\n"
    "v_mfma_f32_16x16x32_bf16 v[148:151], v[108:111], v[92:95], v[148:151]\n"
    "v_cvt_pk_bf16_f32 v247, 0, v247\n"
    "v_add_f32 v124, v124, v244\n"
    "v_mfma_f32_16x16x32_bf16 v[48:51], v[112:115], v[92:95], v[48:51]\n"
    "ds_read_b128 v[32:35], v22 offset:3072\n"
    "v_add_f32 v125, v125, v245\n"
    "v_mfma_f32_16x16x32_bf16 v[16:19], v[132:135], v[40:43], 0\n"
    "v_add_f32 v126, v126, v246\n"
    "v_add_f32 v127, v127, v247\n"
    "s_waitcnt lgkmcnt(4)\n"
    "v_mfma_f32_16x16x32_bf16 v[148:151], v[116:119], v[228:231], v[148:151]\n"
    "v_cvt_pk_bf16_f32 v26, v124, v125\n"
    "v_cvt_pk_bf16_f32 v27, v126, v127\n"
    "v_mfma_f32_16x16x32_bf16 v[48:51], v[120:123], v[228:231], v[48:51]\n"
    "ds_write_b64 v24, v[26:27] offset:2048\n"
    "v_cvt_pk_bf16_f32 v172, v188, v189\n"
    "v_mfma_f32_16x16x32_bf16 v[92:95], v[132:135], v[44:47], 0\n"
    "ds_read_b128 v[224:227], v22 offset:3136\n"
    "v_cvt_pk_bf16_f32 v173, v190, v191\n"
    "v_mfma_f32_16x16x32_bf16 v[148:151], v[236:239], v[196:199], v[148:151]\n"
    "v_cvt_pk_bf16_f32 v174, v16, v17\n"
    "v_cvt_pk_bf16_f32 v175, v18, v19\n"
    "v_mfma_f32_16x16x32_bf16 v[148:151], v[96:99], v[68:71], v[148:151]\n"
    "ds_read2_b32 v[232:233], v23 offset0:8 offset1:9\n"
    "ds_read2_b32 v[234:235], v23 offset0:96 offset1:97\n"
    "v_mfma_f32_16x16x32_bf16 v[148:151], v[192:195], v[208:211], v[148:151]\n"
    "ds_read_b128 v[124:127], v22 offset:1552\n"
    "v_cvt_pk_bf16_f32 v228, v92, v93\n"
    "v_mfma_f32_16x16x32_bf16 v[148:151], v[80:83], v[200:203], v[148:151]\n"
    "s_waitcnt lgkmcnt(4)\n"
    "s_barrier\n"
    "s_setprio 1\n"
    "ds_read_b64_tr_b16 v[132:133], v21 offset:1536\n"
    "v_mfma_f32_16x16x32_bf16 v[148:151], v[100:103], v[216:219], v[148:151]\n"
    "ds_read_b64_tr_b16 v[134:135], v21 offset:36608\n"
    "v_cvt_pk_bf16_f32 v229, v94, v95\n"
    "v_mfma_f32_16x16x32_bf16 v[148:151], v[136:139], v[220:223], v[148:151]\n"
    "v_mfma_f32_16x16x32_bf16 v[148:151], v[104:107], v[212:215], v[148:151]\n"
    "v_mfma_f32_16x16x32_bf16 v[148:151], v[76:79], v[72:75], v[148:151]\n"
    "v_mfma_f32_16x16x32_bf16 v[148:151], v[56:59], v[8:11], v[148:151]\n"
    "v_mfma_f32_16x16x32_bf16 v[148:151], v[144:147], v[184:187], v[148:151]\n"
    "v_mfma_f32_16x16x32_bf16 v[48:51], v[152:155], v[184:187], v[48:51]\n"
    "ds_read_b128 v[16:19], v22 offset:3200\n"
    "v_mfma_f32_16x16x32_bf16 v[188:191], v[64:67], v[40:43], 0\n"
    "v_mfma_f32_16x16x32_bf16 v[48:51], v[160:163], v[52:55], v[48:51]\n"
    "s_waitcnt lgkmcnt(1)\n"
    "global_store_dwordx4 v29, v[132:135], s[34:35]\n"
    "s_add_u32 s34, s34, 1310720\n"
    "s_addc_u32 s35, s35, 0\n"
    "ds_read_b128 v[100:103], v22 offset:3264\n"
    "v_mfma_f32_16x16x32_bf16 v[148:151], v[156:159], v[52:55], v[148:151]\n"
    "v_mfma_f32_16x16x32_bf16 v[48:51], v[168:171], v[128:131], v[48:51]\n"
    "ds_read_b128 v[92:95], v22 offset:3328\n"
    "v_mfma_f32_16x16x32_bf16 v[148:151], v[164:167], v[128:131], v[148:151]\n"
    "v_mfma_f32_16x16x32_bf16 v[8:11], v[60:63], v[32:35], 0\n"
    "v_cvt_pk_bf16_f32 v230, v188, v189\n"
    "v_cvt_pk_bf16_f32 v231, v190, v191\n"
    "v_mfma_f32_16x16x32_bf16 v[148:151], v[12:15], v[32:35], v[148:151]\n"
    "v_mfma_f32_16x16x32_bf16 v[48:51], v[176:179], v[32:35], v[48:51]\n"
    "v_mfma_f32_16x16x32_bf16 v[64:67], v[180:183], v[32:35], 0\n"
    "ds_read_b128 v[132:135], v22 offset:3392\n"
    "ds_read2_b32 v[136:137], v23 offset0:0 offset1:1\n"
    "v_mfma_f32_16x16x32_bf16 v[48:51], v[4:7], v[224:227], v[48:51]\n"
    "ds_read_b128 v[52:55], v22 offset:3456\n"
    "v_cvt_pk_bf16_f32 v8, 0, v8\n"
    "s_waitcnt lgkmcnt(5)\n"
    "v_mfma_f32_16x16x32_bf16 v[48:51], v[84:87], v[16:19], v[48:51]\n"
    "s_setprio 0\n"
    "ds_read_b128 v[128:131], v22 offset:3520\n"
    "v_cvt_pk_bf16_f32 v184, 0, v148\n"
    "v_cvt_pk_bf16_f32 v185, 0, v149\n"
    "v_cvt_pk_bf16_f32 v186, 0, v150\n"
    "s_waitcnt lgkmcnt(5)\n"
    "v_mfma_f32_16x16x32_bf16 v[48:51], v[108:111], v[100:103], v[48:51]\n"
    "ds_read_b128 v[32:35], v22 offset:3584\n"
    "v_cvt_pk_bf16_f32 v187, 0, v151\n"
    "v_cvt_pk_bf16_f32 v9, 0, v9\n"
    "v_cvt_pk_bf16_f32 v10, 0, v10\n"
    "s_waitcnt lgkmcnt(5)\n"
    "v_mfma_f32_16x16x32_bf16 v[48:51], v[116:119], v[92:95], v[48:51]\n"
    "ds_read_b128 v[180:183], v22 offset:3072\n"
    "v_cvt_pk_bf16_f32 v11, 0, v11\n"
    "v_mfma_f32_16x16x32_bf16 v[48:51], v[172:175], v[196:199], v[48:51]\n"
    "ds_read_b128 v[188:191], v22 offset:3056\n"
    "v_add_f32 v184, v184, v8\n"
    "v_mfma_f32_16x16x32_bf16 v[48:51], v[228:231], v[68:71], v[48:51]\n"
    "ds_read_b128 v[240:243], v22 offset:4096\n"
    "v_add_f32 v185, v185, v9\n"
    "v_mfma_f32_16x16x32_bf16 v[48:51], v[236:239], v[208:211], v[48:51]\n"
    "v_add_f32 v186, v186, v10\n"
    "v_add_f32 v187, v187, v11\n"
    "v_mfma_f32_16x16x32_bf16 v[48:51], v[96:99], v[200:203], v[48:51]\n"
    "v_cvt_pk_bf16_f32 v26, v184, v185\n"
    "v_cvt_pk_bf16_f32 v27, v186, v187\n"
    "v_mfma_f32_16x16x32_bf16 v[48:51], v[192:195], v[216:219], v[48:51]\n"
    "ds_write_b64 v24, v[26:27] offset:2560\n"
    "ds_read2_b32 v[138:139], v23 offset0:88 offset1:89\n"
    "v_mfma_f32_16x16x32_bf16 v[48:51], v[80:83], v[220:223], v[48:51]\n"
    "ds_read_b128 v[148:151], v22 offset:2064\n"
    "v_mfma_f32_16x16x32_bf16 v[48:51], v[0:3], v[212:215], v[48:51]\n"
    "v_mfma_f32_16x16x32_bf16 v[48:51], v[104:107], v[72:75], v[48:51]\n"
    "v_mfma_f32_16x16x32_bf16 v[48:51], v[76:79], v[232:235], v[48:51]\n"
    "s_waitcnt lgkmcnt(2)\n"
    "s_barrier\n"
    "s_setprio 1\n"
    "ds_read_b64_tr_b16 v[8:9], v21 offset:2560\n"
    "v_mfma_f32_16x16x32_bf16 v[48:51], v[56:59], v[124:127], v[48:51]\n"
    "ds_read_b64_tr_b16 v[10:11], v21 offset:37632\n"
    "v_mfma_f32_16x16x32_bf16 v[48:51], v[144:147], v[132:135], v[48:51]\n"
    "ds_read_b128 v[184:187], v22 offset:3648\n"
    "v_mfma_f32_16x16x32_bf16 v[48:51], v[156:159], v[52:55], v[48:51]\n"
    "ds_read_b128 v[80:83], v22 offset:3712\n"
    "v_mfma_f32_16x16x32_bf16 v[48:51], v[164:167], v[128:131], v[48:51]\n"
    "ds_read_b128 v[192:195], v22 offset:3776\n"
    "v_mfma_f32_16x16x32_bf16 v[244:247], v[60:63], v[32:35], 0\n"
    "v_mfma_f32_16x16x32_bf16 v[48:51], v[12:15], v[32:35], v[48:51]\n"
    "ds_read_b128 v[248:251], v22 offset:3840\n"
    "v_mfma_f32_16x16x32_bf16 v[64:67], v[88:91], v[224:227], v[64:67]\n"
    "v_mfma_f32_16x16x32_bf16 v[64:67], v[140:143], v[16:19], v[64:67]\n"
    "v_mfma_f32_16x16x32_bf16 v[64:67], v[112:115], v[100:103], v[64:67]\n"
    "s_waitcnt lgkmcnt(4)\n"
    "global_store_dwordx4 v29, v[8:11], s[34:35]\n"
    "s_add_u32 s34, s34, 1310720\n"
    "s_addc_u32 s35, s35, 0\n"
    "v_mfma_f32_16x16x32_bf16 v[64:67], v[120:123], v[92:95], v[64:67]\n"
    "v_mfma_f32_16x16x32_bf16 v[64:67], v[152:155], v[132:135], v[64:67]\n"
    "v_cvt_pk_bf16_f32 v244, 0, v244\n"
    "v_cvt_pk_bf16_f32 v124, 0, v48\n"
    "v_mfma_f32_16x16x32_bf16 v[64:67], v[160:163], v[52:55], v[64:67]\n"
    "v_cvt_pk_bf16_f32 v125, 0, v49\n"
    "v_cvt_pk_bf16_f32 v126, 0, v50\n"
    "v_mfma_f32_16x16x32_bf16 v[64:67], v[168:171], v[128:131], v[64:67]\n"
    "s_setprio 0\n"
    "v_cvt_pk_bf16_f32 v127, 0, v51\n"
    "v_cvt_pk_bf16_f32 v245, 0, v245\n"
    "v_mfma_f32_16x16x32_bf16 v[64:67], v[176:179], v[32:35], v[64:67]\n"
    "v_cvt_pk_bf16_f32 v246, 0, v246\n"
    "v_cvt_pk_bf16_f32 v247, 0, v247\n"
    "v_mfma_f32_16x16x32_bf16 v[8:11], v[180:183], v[36:39], 0\n"
    "v_add_f32 v124, v124, v244\n"
    "v_add_f32 v125, v125, v245\n"
    "v_mfma_f32_16x16x32_bf16 v[16:19], v[180:183], v[40:43], 0\n"
    "v_add_f32 v126, v126, v246\n"
    "v_add_f32 v127, v127, v247\n"
    "v_mfma_f32_16x16x32_bf16 v[52:55], v[180:183], v[44:47], 0\n"
    "v_cvt_pk_bf16_f32 v26, v124, v125\n"
    "v_cvt_pk_bf16_f32 v27, v126, v127\n"
    "v_mfma_f32_16x16x32_bf16 v[32:35], v[188:191], v[40:43], 0\n"
    "ds_write_b64 v24, v[26:27] offset:3072\n"
    "v_cvt_pk_bf16_f32 v48, v8, v9\n"
    "v_mfma_f32_16x16x32_bf16 v[64:67], v[12:15], v[240:243], v[64:67]\n"
    "ds_read_b128 v[36:39], v22 offset:3904\n"
    "v_cvt_pk_bf16_f32 v49, v10, v11\n"
    "s_waitcnt lgkmcnt(5)\n"
    "v_mfma_f32_16x16x32_bf16 v[64:67], v[4:7], v[184:187], v[64:67]\n"
    "ds_read_b128 v[88:91], v22 offset:3968\n"
    "v_cvt_pk_bf16_f32 v50, v16, v17\n"
    "v_mfma_f32_16x16x32_bf16 v[44:47], v[60:63], v[240:243], 0\n"
    "v_cvt_pk_bf16_f32 v51, v18, v19\n"
    "s_waitcnt lgkmcnt(2)\n"
    "s_barrier\n"
    "s_setprio 1\n"
    "v_mfma_f32_16x16x32_bf16 v[64:67], v[84:87], v[80:83], v[64:67]\n"
    "ds_read_b128 v[40:43], v22 offset:4032\n"
    "v_cvt_pk_bf16_f32 v12, v52, v53\n"
    "v_mfma_f32_16x16x32_bf16 v[64:67], v[108:111], v[192:195], v[64:67]\n"
    "v_cvt_pk_bf16_f32 v13, v54, v55\n"
    "v_cvt_pk_bf16_f32 v14, v32, v33\n"
    "v_mfma_f32_16x16x32_bf16 v[64:67], v[48:51], v[196:199], v[64:67]\n"
    "v_cvt_pk_bf16_f32 v15, v34, v35\n"
    "v_cvt_pk_bf16_f32 v44, 0, v44\n"
    "v_cvt_pk_bf16_f32 v45, 0, v45\n"
    "v_mfma_f32_16x16x32_bf16 v[64:67], v[12:15], v[68:71], v[64:67]\n"
    "v_cvt_pk_bf16_f32 v46, 0, v46\n"
    "v_cvt_pk_bf16_f32 v47, 0, v47\n"
    "v_mfma_f32_16x16x32_bf16 v[64:67], v[172:175], v[208:211], v[64:67]\n"
    "v_mfma_f32_16x16x32_bf16 v[64:67], v[228:231], v[200:203], v[64:67]\n"
    "v_mfma_f32_16x16x32_bf16 v[64:67], v[236:239], v[216:219], v[64:67]\n"
    "v_mfma_f32_16x16x32_bf16 v[64:67], v[96:99], v[220:223], v[64:67]\n"
    "v_mfma_f32_16x16x32_bf16 v[64:67], v[204:207], v[212:215], v[64:67]\n"
    "s_setprio 0\n"
    "v_mfma_f32_16x16x32_bf16 v[64:67], v[0:3], v[72:75], v[64:67]\n"
    "v_mfma_f32_16x16x32_bf16 v[64:67], v[104:107], v[232:235], v[64:67]\n"
    "v_mfma_f32_16x16x32_bf16 v[64:67], v[76:79], v[136:139], v[64:67]\n"
    "v_mfma_f32_16x16x32_bf16 v[64:67], v[56:59], v[148:151], v[64:67]\n"
    "v_mfma_f32_16x16x32_bf16 v[64:67], v[116:119], v[248:251], v[64:67]\n"
    "s_waitcnt lgkmcnt(2)\n"
    "v_mfma_f32_16x16x32_bf16 v[64:67], v[144:147], v[36:39], v[64:67]\n"
    "s_waitcnt lgkmcnt(1)\n"
    "v_mfma_f32_16x16x32_bf16 v[64:67], v[156:159], v[88:91], v[64:67]\n"
    "s_waitcnt lgkmcnt(0)\n"
    "v_mfma_f32_16x16x32_bf16 v[64:67], v[164:167], v[40:43], v[64:67]\n"
    "s_nop 7\n"
    "v_cvt_pk_bf16_f32 v4, 0, v64\n"
    "v_add_f32 v4, v4, v44\n"
    "v_cvt_pk_bf16_f32 v5, 0, v65\n"
    "v_add_f32 v5, v5, v45\n"
    "v_cvt_pk_bf16_f32 v22, v4, v5\n"
    "v_cvt_pk_bf16_f32 v6, 0, v66\n"
    "v_add_f32 v6, v6, v46\n"
    "v_cvt_pk_bf16_f32 v7, 0, v67\n"
    "v_add_f32 v7, v7, v47\n"
    "v_cvt_pk_bf16_f32 v23, v6, v7\n"
    "ds_write_b64 v24, v[22:23] offset:3584\n"
    "s_waitcnt lgkmcnt(0)\n"
    "s_barrier\n"
    "ds_read_b64_tr_b16 v[0:1], v21 offset:3584\n"
    "ds_read_b64_tr_b16 v[2:3], v21 offset:38656\n"
    "s_waitcnt lgkmcnt(0)\n"
    "global_store_dwordx4 v29, v[0:3], s[34:35]\n"
    "s_add_u32 s34, s34, 1310720\n"
    "s_addc_u32 s35, s35, 0\n"
    : "+{v0}"(tid)
    : "s"(__builtin_amdgcn_kernarg_segment_ptr()), "s"(blockIdx.x), "s"((unsigned)(size_t)smem)
    : "v1", "v2", "v3", "v4", "v5", "v6", "v7", "v8", "v9", "v10", "v11", "v12", "v13", "v14", "v15", "v16", "v17", "v18", "v19", "v20", "v21", "v22", "v23", "v24", "v25", "v26", "v27", "v28", "v29", "v30", "v31", "v32", "v33", "v34", "v35", "v36", "v37", "v38", "v39", "v40", "v41", "v42", "v43", "v44", "v45", "v46", "v47", "v48", "v49", "v50", "v51", "v52", "v53", "v54", "v55", "v56", "v57", "v58", "v59", "v60", "v61", "v62", "v63", "v64", "v65", "v66", "v67", "v68", "v69", "v70", "v71", "v72", "v73", "v74", "v75", "v76", "v77", "v78", "v79", "v80", "v81", "v82", "v83", "v84", "v85", "v86", "v87", "v88", "v89", "v90", "v91", "v92", "v93", "v94", "v95", "v96", "v97", "v98", "v99", "v100", "v101", "v102", "v103", "v104", "v105", "v106", "v107", "v108", "v109", "v110", "v111", "v112", "v113", "v114", "v115", "v116", "v117", "v118", "v119", "v120", "v121", "v122", "v123", "v124", "v125", "v126", "v127", "v128", "v129", "v130", "v131", "v132", "v133", "v134", "v135", "v136", "v137", "v138", "v139", "v140", "v141", "v142", "v143", "v144", "v145", "v146", "v147", "v148", "v149", "v150", "v151", "v152", "v153", "v154", "v155", "v156", "v157", "v158", "v159", "v160", "v161", "v162", "v163", "v164", "v165", "v166", "v167", "v168", "v169", "v170", "v171", "v172", "v173", "v174", "v175", "v176", "v177", "v178", "v179", "v180", "v181", "v182", "v183", "v184", "v185", "v186", "v187", "v188", "v189", "v190", "v191", "v192", "v193", "v194", "v195", "v196", "v197", "v198", "v199", "v200", "v201", "v202", "v203", "v204", "v205", "v206", "v207", "v208", "v209", "v210", "v211", "v212", "v213", "v214", "v215", "v216", "v217", "v218", "v219", "v220", "v221", "v222", "v223", "v224", "v225", "v226", "v227", "v228", "v229", "v230", "v231", "v232", "v233", "v234", "v235", "v236", "v237", "v238", "v239", "v240", "v241", "v242", "v243", "v244", "v245", "v246", "v247", "v248", "v249", "v250", "v251", "s8", "s9", "s10", "s11", "s12", "s13", "s14", "s15", "s16", "s17", "s18", "s19", "s20", "s21", "s22", "s23", "s24", "s25", "s26", "s27", "s28", "s29", "s30", "s31", "s33", "s34", "s35", "s36", "s37", "s38", "s39", "s40", "s41", "s42", "s43", "s44", "s45", "s46", "s47", "s48", "s49", "s50", "s51", "s52", "s53", "s54", "s55", "s56", "s57", "s58", "s59", "vcc", "scc", "memory");
}

extern "C" void kernel_launch(void* const* d_in, const int* in_sizes, int n_in,
                              void* d_out, int out_size, void* d_ws, size_t ws_size,
                              hipStream_t stream) {
    Args A;
    const int ia[8] = {1, 2, 5, 3, 4, 3, 4, 0}, ib[8] = {8, 9, 14, 12, 13, 10, 11, 7};
    for (int i = 0; i < 8; ++i) { A.p[0][i] = d_in[ia[i]]; A.p[1][i] = d_in[ib[i]]; }
    A.skip[0] = d_in[6]; A.skip[1] = d_in[15]; A.out = d_out;
    ssm_kernel<<<256, 512, 0, stream>>>(A);
}
```
